```python
import torch

def _softmax(x, axis=-1):
    return torch.softmax(x, dim=axis)

def _gelu(x):
    return torch.nn.functional.gelu(x, approximate='tanh')

def _silu(x):
    return torch.nn.functional.silu(x)

def _sigmoid(x):
    return torch.sigmoid(x)

def _layernorm(x, axis=-1, eps=1e-05):
    m = x.mean(dim=axis, keepdim=True)
    v = x.var(dim=axis, keepdim=True, unbiased=False)
    return (x - m) / torch.sqrt(v + eps)

def _scatter_add(tgt, idx, src, axis=0):
    return tgt.clone().index_add_(axis, idx, src)

def _segment_sum(src, seg, n):
    out = torch.zeros((n,) + src.shape[1:], dtype=src.dtype)
    return out.index_add_(0, seg, src)

def _pad(x, pw):
    flat = tuple((v for pair in reversed(pw) for v in pair))
    return torch.nn.functional.pad(x, flat)
kernel_name = 'gated_mix__memory_aug_recurrent_mem_tr__memory_aug_recurrent_mem_tr__prompt'

def setup_inputs():

    _w = lambda a: (lambda t: t.to(torch.bfloat16) if t.is_floating_point() else t)(torch.as_tensor(a))

    def _A_setup():
        B, S, Dm = (1, 2048, 576)
        H, Dh = (32, 128)
        M, seg = (8, 32)
        seg = min(seg, S)
        S = S // seg * seg
        return {'x': _w((lambda *s: torch.randn(*s, dtype=torch.bfloat16) * 0.05)(B, S, Dm)), 'mem0': _w((lambda *s: torch.randn(*s, dtype=torch.bfloat16) * 0.4)(B, M, Dm)), 'Wq': _w((lambda *s: torch.randn(*s, dtype=torch.bfloat16) * 6.4)(Dm, H, Dh) * Dm ** (-0.5)), 'Wk': _w((lambda *s: torch.randn(*s, dtype=torch.bfloat16) * 6.4)(Dm, H, Dh) * Dm ** (-0.5)), 'Wv': _w((lambda *s: torch.randn(*s, dtype=torch.bfloat16) * 0.4)(Dm, H, Dh) * Dm ** (-0.5)), 'Wo': _w((lambda *s: torch.randn(*s, dtype=torch.bfloat16) * 0.4)(H, Dh, Dm) * (H * Dh) ** (-0.5)), 'seg_len': seg}
    _A_in = _A_setup()

    def _B_setup():
        B, S, Dm = (1, 2048, 576)
        H, Dh = (32, 128)
        M, seg = (8, 32)
        seg = min(seg, S)
        S = S // seg * seg
        return {'x': _w((lambda *s: torch.randn(*s, dtype=torch.bfloat16) * 0.05)(B, S, Dm)), 'mem0': _w((lambda *s: torch.randn(*s, dtype=torch.bfloat16) * 0.4)(B, M, Dm)), 'Wq': _w((lambda *s: torch.randn(*s, dtype=torch.bfloat16) * 6.4)(Dm, H, Dh) * Dm ** (-0.5)), 'Wk': _w((lambda *s: torch.randn(*s, dtype=torch.bfloat16) * 6.4)(Dm, H, Dh) * Dm ** (-0.5)), 'Wv': _w((lambda *s: torch.randn(*s, dtype=torch.bfloat16) * 0.4)(Dm, H, Dh) * Dm ** (-0.5)), 'Wo': _w((lambda *s: torch.randn(*s, dtype=torch.bfloat16) * 0.4)(H, Dh, Dm) * (H * Dh) ** (-0.5)), 'seg_len': seg}
    _B_in = _B_setup()
    return {'A_x': _A_in['x'], 'A_mem0': _A_in['mem0'], 'A_Wq': _A_in['Wq'], 'A_Wk': _A_in['Wk'], 'A_Wv': _A_in['Wv'], 'A_Wo': _A_in['Wo'], 'A_seg_len': _A_in['seg_len'], 'B_x': _B_in['x'], 'B_mem0': _B_in['mem0'], 'B_Wq': _B_in['Wq'], 'B_Wk': _B_in['Wk'], 'B_Wv': _B_in['Wv'], 'B_Wo': _B_in['Wo'], 'B_seg_len': _B_in['seg_len'], 'gate_w': _w((lambda *s: torch.randn(*s, dtype=torch.bfloat16) * 0.05)(576) * 16.0)}

def reference(A_x, A_mem0, A_Wq, A_Wk, A_Wv, A_Wo, A_seg_len, B_x, B_mem0, B_Wq, B_Wk, B_Wv, B_Wo, B_seg_len, gate_w):

    def _A_ref(x, mem0, Wq, Wk, Wv, Wo, seg_len):
        B, S, Dm = x.shape
        H, Dh = (Wq.shape[1], Wq.shape[2])
        M = mem0.shape[1]
        scale = Dh ** (-0.5)
        n_seg = S // seg_len
        L = M + seg_len
        idx = torch.arange(L)
        mask = (idx[:, None] < M) | (idx[:, None] >= idx[None, :])
        mem = mem0
        outs = []
        for s in range(n_seg):
            xs = x[:, s * seg_len:(s + 1) * seg_len, :].clone()
            h = torch.concatenate([mem, xs], axis=1)
            q = torch.einsum('btd,dhe->bhte', h, Wq)
            k = torch.einsum('btd,dhe->bhte', h, Wk)
            v = torch.einsum('btd,dhe->bhte', h, Wv)
            scores = torch.einsum('bhqe,bhke->bhqk', q, k) * scale
            scores = torch.where(mask[None, None], scores, -1000000000.0)
            p = _softmax(scores, axis=-1)
            o = torch.einsum('bhqk,bhke->bhqe', p, v)
            o = torch.einsum('bhte,hed->btd', o, Wo)
            mem = o[:, :M, :].clone()
            outs.append(o[:, M:, :])
        return torch.concatenate(outs, axis=1)

    def _B_ref(x, mem0, Wq, Wk, Wv, Wo, seg_len):
        B, S, Dm = x.shape
        H, Dh = (Wq.shape[1], Wq.shape[2])
        M = mem0.shape[1]
        scale = Dh ** (-0.5)
        n_seg = S // seg_len
        L = M + seg_len
        idx = torch.arange(L)
        mask = (idx[:, None] < M) | (idx[:, None] >= idx[None, :])
        mem = mem0
        outs = []
        for s in range(n_seg):
            xs = x[:, s * seg_len:(s + 1) * seg_len, :].clone()
            h = torch.concatenate([mem, xs], axis=1)
            q = torch.einsum('btd,dhe->bhte', h, Wq)
            k = torch.einsum('btd,dhe->bhte', h, Wk)
            v = torch.einsum('btd,dhe->bhte', h, Wv)
            scores = torch.einsum('bhqe,bhke->bhqk', q, k) * scale
            scores = torch.where(mask[None, None], scores, -1000000000.0)
            p = _softmax(scores, axis=-1)
            o = torch.einsum('bhqk,bhke->bhqe', p, v)
            o = torch.einsum('bhte,hed->btd', o, Wo)
            mem = o[:, :M, :].clone()
            outs.append(o[:, M:, :])
        return torch.concatenate(outs, axis=1)
    x = A_x
    g = _sigmoid((x * gate_w).sum(-1, keepdims=True))
    a = _A_ref(x, A_mem0, A_Wq, A_Wk, A_Wv, A_Wo, A_seg_len)
    b = _B_ref(x, B_mem0, B_Wq, B_Wk, B_Wv, B_Wo, B_seg_len)
    return g * a + (1 - g) * b
```

```cpp
#include <hip/hip_runtime.h>
#include <hip/hip_bf16.h>
#include <stdint.h>

namespace {

typedef __attribute__((ext_vector_type(2))) __bf16 bf16x2;
typedef __attribute__((ext_vector_type(4))) __bf16 bf16x4;
typedef __attribute__((ext_vector_type(8))) __bf16 bf16x8;
typedef __attribute__((ext_vector_type(16))) __bf16 bf16x16;
typedef __attribute__((ext_vector_type(4))) short s16x4;
typedef __attribute__((ext_vector_type(2))) float f32x2;
typedef __attribute__((ext_vector_type(4))) float f32x4;
typedef __attribute__((ext_vector_type(2))) unsigned u32x2;
typedef __attribute__((ext_vector_type(4))) unsigned u32x4;
#define LAS __attribute__((address_space(3)))
#define GAS __attribute__((address_space(1)))

constexpr int DM = 576, NH = 32, DH = 128, NSEG = 64;
constexpr int LDS_BYTES = 163840;

constexpr int CW = 32;
constexpr int C_TMO = 0;
constexpr int C_PAR = 1;
constexpr int C_INIT = 3;
constexpr int C_KV = 8;
constexpr int C_Q = 72;
constexpr int C_OX = 104;
constexpr int C_PART = 232;
constexpr int C_END = 328;
constexpr size_t CTL_BYTES = (size_t)C_END * CW * 4;

constexpr size_t FRAGSEG = 8192;
constexpr size_t FRAGALL = (size_t)2 * NH * NSEG * FRAGSEG;
constexpr size_t PARTSEG = (size_t)18 * 1024;

struct Params {
  const __bf16* x[2];
  const __bf16* mem0[2];
  const __bf16* wq[2];
  const __bf16* wk[2];
  const __bf16* wv[2];
  const __bf16* wo[2];
  const __bf16* gate;
  __bf16* out;
  unsigned* ctl;
  char* pageP[2];
  char* pageM[2];
  char* kx;
  char* vx;
  char* qx;
  char* ox;
  char* part;
};

constexpr unsigned SPIN_CAP = 60000;
#define DELAY1 300u
#define AHEAD1 212u
#define AHEAD3 0u
#define AHEAD1S 300u
#define AHEAD2 31u
#define DELAY2 1000u

__device__ __forceinline__ unsigned pk2(float a, float b) {
  f32x2 x = {a, b};
  return __builtin_bit_cast(unsigned, __builtin_convertvector(x, bf16x2));
}
template <int LANE>
__device__ __forceinline__ void put_lane(unsigned& v, unsigned x) {
  asm("s_nop 1\n\tv_writelane_b32 %0, %1, %2" : "+v"(v) : "s"(__builtin_amdgcn_readfirstlane(x)), "i"(LANE));
}
template <typename T>
__device__ __forceinline__ T* uniform_ptr(T* p) {
  const uint64_t x = (uint64_t)p;
  return (T*)(((uint64_t)(unsigned)__builtin_amdgcn_readfirstlane((unsigned)(x >> 32)) << 32) | (unsigned)__builtin_amdgcn_readfirstlane((unsigned)x));
}
__device__ __forceinline__ unsigned umin32(unsigned a, unsigned b) { return a < b ? a : b; }
__device__ __forceinline__ void wg_barrier() {
  asm volatile("s_waitcnt lgkmcnt(0)" ::: "memory");
  __builtin_amdgcn_s_barrier();
  asm volatile("" ::: "memory");
}
__device__ __forceinline__ void vm_drain() { asm volatile("s_waitcnt vmcnt(0)" ::: "memory"); }
__device__ __forceinline__ unsigned ld_flag(const unsigned* p) {
  return __hip_atomic_load((GAS const unsigned*)p, __ATOMIC_RELAXED, __HIP_MEMORY_SCOPE_AGENT);
}
__device__ __forceinline__ void st_flag(unsigned* p, unsigned v) {
  __hip_atomic_store((GAS unsigned*)p, v, __ATOMIC_RELAXED, __HIP_MEMORY_SCOPE_AGENT);
}
__device__ __forceinline__ void wait_ge(const unsigned* p, unsigned want, unsigned* ctl) {
  for (unsigned sp = 0;; ++sp) {
    asm volatile("" ::: "memory");
    if (__builtin_amdgcn_readfirstlane(ld_flag(p)) >= want) break;
    if ((sp & 63u) == 63u) {
      if (__builtin_amdgcn_readfirstlane(ld_flag(ctl + C_TMO * CW))) break;
      if (sp > SPIN_CAP) { st_flag(ctl + C_TMO * CW, 1u); break; }
    }
    __builtin_amdgcn_s_sleep(2);
  }
}
template <bool SC1>
__device__ __forceinline__ void glds16(const void* gsrc, unsigned lds_dst) {
  unsigned keep;
  if (SC1)
    asm volatile("s_mov_b32 %0, m0\n\ts_mov_b32 m0, %2\n\ts_nop 0\n\tglobal_load_lds_dwordx4 %1, off sc1\n\ts_mov_b32 m0, %0"
                 : "=&s"(keep) : "v"(gsrc), "s"(lds_dst) : "memory");
  else
    asm volatile("s_mov_b32 %0, m0\n\ts_mov_b32 m0, %2\n\ts_nop 0\n\tglobal_load_lds_dwordx4 %1, off\n\ts_mov_b32 m0, %0"
                 : "=&s"(keep) : "v"(gsrc), "s"(lds_dst) : "memory");
}
template <bool SC1>
__device__ __forceinline__ void glds16s(const char* sbase, unsigned voff, unsigned lds_dst) {
  unsigned keep;
  if (SC1)
    asm volatile("s_mov_b32 %0, m0\n\ts_mov_b32 m0, %2\n\ts_nop 0\n\tglobal_load_lds_dwordx4 %1, %3 sc1\n\ts_mov_b32 m0, %0"
                 : "=&s"(keep) : "v"(voff), "s"(lds_dst), "s"(sbase) : "memory");
  else
    asm volatile("s_mov_b32 %0, m0\n\ts_mov_b32 m0, %2\n\ts_nop 0\n\tglobal_load_lds_dwordx4 %1, %3\n\ts_mov_b32 m0, %0"
                 : "=&s"(keep) : "v"(voff), "s"(lds_dst), "s"(sbase) : "memory");
}
__device__ __forceinline__ bf16x4 tr_read(unsigned addr) {
  return __builtin_amdgcn_ds_read_tr16_b64_v4bf16((LAS bf16x4*)(uintptr_t)addr);
}
__device__ __forceinline__ bf16x8 cat4(bf16x4 a, bf16x4 b) { return __builtin_shufflevector(a, b, 0, 1, 2, 3, 4, 5, 6, 7); }
__device__ __forceinline__ bf16x16 cat8(bf16x8 a, bf16x8 b) {
  return __builtin_shufflevector(a, b, 0, 1, 2, 3, 4, 5, 6, 7, 8, 9, 10, 11, 12, 13, 14, 15);
}
#define SB() __builtin_amdgcn_sched_barrier(0)
template <int V> struct IC { static constexpr int value = V; };
template <int N, int I = 0, typename F>
__device__ __forceinline__ void static_for(F&& f) {
  if constexpr (I < N) { f(IC<I>{}); static_for<N, I + 1>(f); }
}
template <bool NOP2 = false>
__device__ __forceinline__ void smf_a(f32x4& acc, const bf16x8& A, const bf16x16& W, int idx) {
  if (NOP2) asm volatile("s_nop 1\n\tv_smfmac_f32_16x16x64_bf16 %0, %1, %2, %3" : "+v"(acc) : "v"(A), "a"(W), "v"(idx));
  else asm volatile("v_smfmac_f32_16x16x64_bf16 %0, %1, %2, %3" : "+v"(acc) : "v"(A), "a"(W), "v"(idx));
}
template <bool NOP2 = false>
__device__ __forceinline__ void smf_v(f32x4& acc, const bf16x8& A, const bf16x16& W, int idx) {
  if (NOP2) asm volatile("s_nop 1\n\tv_smfmac_f32_16x16x64_bf16 %0, %1, %2, %3" : "+v"(acc) : "v"(A), "v"(W), "v"(idx));
  else asm volatile("v_smfmac_f32_16x16x64_bf16 %0, %1, %2, %3" : "+v"(acc) : "v"(A), "v"(W), "v"(idx));
}
template <bool AG, int R, int OFF>
__device__ __forceinline__ void tr_read_lit(unsigned addr) {
  if constexpr (AG) asm volatile("ds_read_b64_tr_b16 a[%c1:%c2], %0 offset:%c3" ::"v"(addr), "i"(R), "i"(R + 1), "i"(OFF) : "memory");
  else asm volatile("ds_read_b64_tr_b16 v[%c1:%c2], %0 offset:%c3" ::"v"(addr), "i"(R), "i"(R + 1), "i"(OFF) : "memory");
}
template <bool AG, int R>
__device__ __forceinline__ void smf_lit(f32x4& acc, const bf16x8& A, int idx) {
  if constexpr (AG) asm volatile("v_smfmac_f32_16x16x64_bf16 %0, %1, a[%c3:%c4], %2" : "+v"(acc) : "v"(A), "v"(idx), "i"(R), "i"(R + 7));
  else asm volatile("v_smfmac_f32_16x16x64_bf16 %0, %1, v[%c3:%c4], %2" : "+v"(acc) : "v"(A), "v"(idx), "i"(R), "i"(R + 7));
}
template <bool AG, int R, bool WA, bool FIRST>
__device__ __forceinline__ void mfma_lit(f32x4& acc, const bf16x8& X) {
  if constexpr (FIRST) {
    if constexpr (AG && WA) asm volatile("v_mfma_f32_16x16x32_bf16 %0, a[%c2:%c3], %1, 0" : "=&v"(acc) : "v"(X), "i"(R), "i"(R + 3));
    else if constexpr (AG) asm volatile("v_mfma_f32_16x16x32_bf16 %0, %1, a[%c2:%c3], 0" : "=&v"(acc) : "v"(X), "i"(R), "i"(R + 3));
    else if constexpr (WA) asm volatile("v_mfma_f32_16x16x32_bf16 %0, v[%c2:%c3], %1, 0" : "=&v"(acc) : "v"(X), "i"(R), "i"(R + 3));
    else asm volatile("v_mfma_f32_16x16x32_bf16 %0, %1, v[%c2:%c3], 0" : "=&v"(acc) : "v"(X), "i"(R), "i"(R + 3));
  } else {
    if constexpr (AG && WA) asm volatile("v_mfma_f32_16x16x32_bf16 %0, a[%c2:%c3], %1, %0" : "+v"(acc) : "v"(X), "i"(R), "i"(R + 3));
    else if constexpr (AG) asm volatile("v_mfma_f32_16x16x32_bf16 %0, %1, a[%c2:%c3], %0" : "+v"(acc) : "v"(X), "i"(R), "i"(R + 3));
    else if constexpr (WA) asm volatile("v_mfma_f32_16x16x32_bf16 %0, v[%c2:%c3], %1, %0" : "+v"(acc) : "v"(X), "i"(R), "i"(R + 3));
    else asm volatile("v_mfma_f32_16x16x32_bf16 %0, %1, v[%c2:%c3], %0" : "+v"(acc) : "v"(X), "i"(R), "i"(R + 3));
  }
}
template <bool SC1>
__device__ __forceinline__ void glds16f(const char* sbase, unsigned voff, unsigned lds_dst) {
  if (SC1) asm volatile("s_mov_b32 m0, %1\n\ts_nop 0\n\tglobal_load_lds_dwordx4 %0, %2 sc1" ::"v"(voff), "s"(lds_dst), "s"(sbase) : "memory");
  else asm volatile("s_mov_b32 m0, %1\n\ts_nop 0\n\tglobal_load_lds_dwordx4 %0, %2" ::"v"(voff), "s"(lds_dst), "s"(sbase) : "memory");
}
template <int R, int IMM>
__device__ __forceinline__ void ld16_sc1_lit(const char* base, unsigned off) {
  asm volatile("global_load_dwordx4 v[%c2:%c3], %0, %1 offset:%c4 sc1" ::"v"(off), "s"(base), "i"(R), "i"(R + 3), "i"(IMM) : "memory");
}
#define DEF_MFMA(NAME, CA, CB)                                                                                                        \
  template <bool FIRST, bool NOP2 = false>                                                                                            \
  __device__ __forceinline__ void NAME(f32x4& acc, const bf16x8& A, const bf16x8& B) {                                                \
    if (FIRST) {                                                                                                                      \
      if (NOP2) asm volatile("s_nop 1\n\tv_mfma_f32_16x16x32_bf16 %0, %1, %2, 0" : "=&v"(acc) : CA(A), CB(B));                        \
      else asm volatile("v_mfma_f32_16x16x32_bf16 %0, %1, %2, 0" : "=&v"(acc) : CA(A), CB(B));                                        \
    } else {                                                                                                                          \
      if (NOP2) asm volatile("s_nop 1\n\tv_mfma_f32_16x16x32_bf16 %0, %1, %2, %0" : "+v"(acc) : CA(A), CB(B));                        \
      else asm volatile("v_mfma_f32_16x16x32_bf16 %0, %1, %2, %0" : "+v"(acc) : CA(A), CB(B));                                        \
    }                                                                                                                                 \
  }
DEF_MFMA(mfma_vv, "v", "v")
DEF_MFMA(mfma_av, "a", "v")
DEF_MFMA(mfma_va, "v", "a")
template <bool NOP2 = false>
__device__ __forceinline__ void mfma16_first(f32x4& acc, const s16x4& A, const bf16x4& B) {
  if (NOP2) asm volatile("s_nop 1\n\tv_mfma_f32_16x16x16_bf16 %0, %1, %2, 0" : "=&v"(acc) : "v"(A), "v"(B));
  else asm volatile("v_mfma_f32_16x16x16_bf16 %0, %1, %2, 0" : "=&v"(acc) : "v"(A), "v"(B));
}
__device__ __forceinline__ void mfma_fence(f32x4& a) { asm volatile("s_nop 7" : "+v"(a)); }
__device__ __forceinline__ void mfma_fence(f32x4& a, f32x4& b) { asm volatile("s_nop 7" : "+v"(a), "+v"(b)); }
__device__ __forceinline__ void mfma_fence(f32x4& a, f32x4& b, f32x4& c) { asm volatile("s_nop 7" : "+v"(a), "+v"(b), "+v"(c)); }
__device__ __forceinline__ void mfma_fence(f32x4& a, f32x4& b, f32x4& c, f32x4& d) { asm volatile("s_nop 7" : "+v"(a), "+v"(b), "+v"(c), "+v"(d)); }
__device__ __forceinline__ void pin_v(f32x4& a) { asm volatile("" : "+v"(a)); }
__device__ __forceinline__ unsigned opaque(unsigned a) { asm("" : "+v"(a)); return a; }
template <typename T>
__device__ __forceinline__ void pin_agpr(T& v) { asm volatile("" : "+a"(v)); }
template <typename T>
__device__ __forceinline__ T lds_ld(unsigned addr) { return *(LAS const T*)(uintptr_t)addr; }
template <typename T>
__device__ __forceinline__ void lds_st(unsigned addr, T v) { *(LAS T*)(uintptr_t)addr = v; }
__device__ __forceinline__ void st16_sc1(char* base, unsigned off, u32x4 v) {
  asm volatile("global_store_dwordx4 %0, %1, %2 sc1\n\ts_nop 1" ::"v"(off), "v"(v), "s"(base) : "memory");
}
template <int IMM = 0>
__device__ __forceinline__ u32x4 ld16_sc1_issue(const char* base, unsigned off) {
  u32x4 v;
  asm volatile("global_load_dwordx4 %0, %1, %2 offset:%3 sc1" : "=v"(v) : "v"(off), "s"(base), "i"(IMM) : "memory");
  return v;
}
__device__ __forceinline__ unsigned lmap(unsigned x, unsigned par) { return ((x >> 13) << 14) + (par << 13) + (x & 8191u); }

__device__ __forceinline__ float xor32_max(float x) {
  unsigned u = __float_as_uint(x);
  auto r = __builtin_amdgcn_permlane32_swap(u, u, false, false);
  return fmaxf(__uint_as_float(r[0]), __uint_as_float(r[1]));
}
__device__ __forceinline__ float xor16_max(float x) {
  unsigned u = __float_as_uint(x);
  auto r = __builtin_amdgcn_permlane16_swap(u, u, false, false);
  return fmaxf(__uint_as_float(r[0]), __uint_as_float(r[1]));
}
__device__ __forceinline__ float xor32_add(float x) {
  unsigned u = __float_as_uint(x);
  auto r = __builtin_amdgcn_permlane32_swap(u, u, false, false);
  return __uint_as_float(r[0]) + __uint_as_float(r[1]);
}
__device__ __forceinline__ float xor16_add(float x) {
  unsigned u = __float_as_uint(x);
  auto r = __builtin_amdgcn_permlane16_swap(u, u, false, false);
  return __uint_as_float(r[0]) + __uint_as_float(r[1]);
}

__device__ __forceinline__ unsigned chunk_lane_off(int w, int lane) {
  const int rr = lane >> 4, ch = lane & 15;
  const int swz = 2 * (rr | (((w >> 1) & 1) << 2));
  return (unsigned)((4 * w + rr) * (NH * DH * 2) + ((ch ^ swz) * 16));
}
__device__ __forceinline__ void stage_chunk(const __bf16* W, int h, int t, int slot, unsigned voff, int w, unsigned ring = 0) {
  const char* src = (const char*)W + ((size_t)t * 64 * (NH * DH) + (size_t)h * DH) * 2;
#pragma unroll
  for (int k = 0; k < 4; ++k) glds16f<false>(src + (size_t)k * 16 * (NH * DH * 2), voff, ring + (unsigned)(slot * 16384 + (w + 4 * k) * 1024));
}
struct WBase { unsigned b0, b1, b2; };
__device__ __forceinline__ WBase w_base(int T, int lane, unsigned ring = 0) {
  const int q = lane >> 4, q2 = (lane & 15) >> 2, p = lane & 3;
  const int swzh = q2 | ((q & 1) << 2);
  const unsigned a = ring + (8 * q + q2) * 256 + (((T ^ swzh) * 2 + (p >> 1)) * 16) + (p & 1) * 8;
  return WBase{opaque(a), opaque(a + 65536), opaque(a + 131072)};
}
template <int SLOT, int HF>
__device__ __forceinline__ bf16x8 read_w32(const WBase& b) {
  const unsigned a = (SLOT < 4 ? b.b0 : SLOT < 8 ? b.b1 : b.b2) + (SLOT & 3) * 16384 + HF * 8192;
  return cat4(tr_read(a), tr_read(a + 1024));
}
template <int SLOT, int HF, bool AG, int R>
__device__ __forceinline__ void read_w32_lit(const WBase& b) {
  constexpr int off = (SLOT & 3) * 16384 + HF * 8192;
  const unsigned a = SLOT < 4 ? b.b0 : SLOT < 8 ? b.b1 : b.b2;
  tr_read_lit<AG, R, off>(a);
  tr_read_lit<AG, R + 2, off + 1024>(a);
}
template <int N> __device__ __forceinline__ void wait_vm() { asm volatile("s_waitcnt vmcnt(%0) lgkmcnt(0)" ::"i"(N) : "memory"); }
template <int NCH, int NS, int XTRA, typename IssueF, typename FillF, typename ConsF>
__device__ __forceinline__ void ring_stage(IssueF issue, FillF after_fill, ConsF consume) {
  static_for<NS>([&](auto ic) { issue(ic, ic); });
  after_fill();
  static_for<NCH>([&](auto ic) {
    constexpr int i = decltype(ic)::value;
    constexpr int last = i == 0 ? NS - 1 : (i - 2 + NS < NCH - 1 ? i - 2 + NS : NCH - 1);
    wait_vm<4 * (last - i) + (i < NS ? XTRA : 0)>();
    __builtin_amdgcn_s_barrier();
    asm volatile("" ::: "memory");
    if constexpr (i >= 1 && i - 1 + NS < NCH) issue(IC<i - 1 + NS>{}, IC<(i - 1) % NS>{});
    consume(ic, IC<i % NS>{});
  });
  wg_barrier();
}

template <int F, int SLOT>
__device__ __forceinline__ void load_w8(const WBase& b) {
  constexpr bool AG = F < 32;
  constexpr int R = AG ? 8 * F : 112 + 8 * (F - 32);
  read_w32_lit<SLOT, 0, AG, R>(b);
  read_w32_lit<SLOT, 1, AG, R + 4>(b);
}
template <int F, bool WA, bool FIRST>
__device__ __forceinline__ void mfma_w(f32x4& acc, const bf16x8& X) {
  mfma_lit<(F < 60), (F < 60 ? 16 + 4 * F : 112 + 4 * (F - 60)), WA, FIRST>(acc, X);
}

template <int RA, int F, bool FIRST>
__device__ __forceinline__ void mfma_ow(f32x4& acc) {
  constexpr int R = F < 60 ? 16 + 4 * F : 112 + 4 * (F - 60);
  if constexpr (F < 60) {
    if constexpr (FIRST) asm volatile("v_mfma_f32_16x16x32_bf16 %0, v[%c1:%c2], a[%c3:%c4], 0" : "=&v"(acc) : "i"(RA), "i"(RA + 3), "i"(R), "i"(R + 3));
    else asm volatile("v_mfma_f32_16x16x32_bf16 %0, v[%c1:%c2], a[%c3:%c4], %0" : "+v"(acc) : "i"(RA), "i"(RA + 3), "i"(R), "i"(R + 3));
  } else {
    if constexpr (FIRST) asm volatile("v_mfma_f32_16x16x32_bf16 %0, v[%c1:%c2], v[%c3:%c4], 0" : "=&v"(acc) : "i"(RA), "i"(RA + 3), "i"(R), "i"(R + 3));
    else asm volatile("v_mfma_f32_16x16x32_bf16 %0, v[%c1:%c2], v[%c3:%c4], %0" : "+v"(acc) : "i"(RA), "i"(RA + 3), "i"(R), "i"(R + 3));
  }
}

constexpr unsigned L_WO = 0;
constexpr unsigned L_WV = 73728;
constexpr unsigned L_KVQ = 106496;
constexpr unsigned KVQ_SZ = 20480;
constexpr unsigned L_MEM = 147456;
constexpr unsigned L_QT = 156672;
constexpr unsigned L_KT = 158720;
constexpr unsigned L_OI = 160768;
constexpr unsigned ST_WO0 = 65536, ST_WO1 = 126976;
constexpr float SM_C = 0.08838834764831845f * 1.4426950408889634f;

#define CHAIN_CLOBBERS "s31", "s33", "s34", "s35", "s36", "s37", "s38", "s39", "v16", "v17", "v18", "v19", "v20", "v21", "v22", "v23", "v24", "v25", "v26", "v27", "v28", "v29", "v30", "v31", "v32", "v33", "v34", "v35", "v36", "v37", "v38", "v39", "v40", "v41", "v42", "v43", "v44", "v45", "v46", "v47", "v48", "v49", "v50", "v51", "v52", "v53", "v54", "v55", "v56", "v57", "v58", "v59", "v60", "v61", "v62", "v63", "v64", "v65", "v66", "v67", "v68", "v69", "v70", "v71", "v72", "v73", "v74", "v75", "v76", "v77", "v78", "v79", "v80", "v81", "v82", "v83", "v84", "v85", "v86", "v87", "v88", "v89", "v90", "v91", "v92", "v93", "v94", "v95", "v96", "v97", "v98", "v99", "v100", "v101", "v102", "v103", "v104", "v105", "v106", "v107", "v108", "v109", "v110", "v111", "s40", "s41", "s42", "s43", "s44", "s45", "s46", "s47", "s48", "s49", "s50", "s51", "s52", "s53", "s54", "s55", "s56", "s57", "s58", "s59", "s60", "s61", "s62", "s63", "s64", "s65", "s66", "s67", "s68", "s69", "s70", "s71", "s72", "s73", "s74", "s75", "s76", "s77", "s78", "s79", "s80", "s81", "s82", "s83", "s84", "s85", "s86", "s87", "s88", "s89", "s90", "s91", "s92", "s93", "s94", "s95", "s96", "s97", "s98", "s99", "s100", "s101", "vcc", "scc", "memory"
__device__ __forceinline__ void chain_role(const Params& P, int g, int c) {
  const int tid = threadIdx.x, lane = tid & 63, w = __builtin_amdgcn_readfirstlane(tid >> 6);
  const int h = c >> 1, j = c & 1;
  unsigned* ctl = P.ctl;
  char* const pageP = P.pageP[g];
  char* const pageM = P.pageM[g];

  const WBase wb0 = w_base(2 * w, lane), wb1 = w_base(2 * w + 1, lane);
  const unsigned cvoff = chunk_lane_off(w, lane);
  {
    const char* tp = w == 0 ? pageP + (1792 << 10) : w == 1 ? pageM + 4096 : w == 2 ? P.ox + (size_t)(g * NH + h) * NSEG * FRAGSEG : (const char*)(ctl + C_END * CW);
    asm volatile("global_load_dword v255, %0, off sc1" ::"v"(tp) : "memory");
  }
  unsigned short m0v[3][8];
#pragma unroll
  for (int k = 0; k < 3; ++k)
#pragma unroll
    for (int m = 0; m < 8; ++m) m0v[k][m] = ((const unsigned short*)P.mem0[g])[m * DM + (tid + 256 * k < DM ? tid + 256 * k : 0)];
  bf16x16 wvt[2][2];
  ring_stage<27, 4, 18>(
      [&](auto ic, auto sc) { constexpr int i = decltype(ic)::value; stage_chunk(i < 9 ? P.wq[g] : i < 18 ? P.wk[g] : P.wv[g], h, i % 9, decltype(sc)::value, cvoff, w); },
      [&]() {
        const char* wo = (const char*)P.wo[g] + ((size_t)h * DH * DM + j * 288) * 2;
        unsigned pc = w * 64 + lane, row = pc / 36, ch = pc - row * 36;
        for (int i = 0; i < 18; ++i) {
          const int ins = w + 4 * i;
          glds16<false>(wo + (size_t)row * (DM * 2) + ch * 16, (ins < 36 ? ST_WO0 : ST_WO1 - 36 * 1024) + ins * 1024);
          ch += 4; row += 7;
          if (ch >= 36) { ch -= 36; row += 1; }
        }
      },
      [&](auto ic, auto sc) {
        constexpr int i = decltype(ic)::value, t = i % 9, slot = decltype(sc)::value;
        if constexpr (i < 9) { load_w8<t, slot>(wb0); load_w8<9 + t, slot>(wb1); }
        else if constexpr (i < 18) { load_w8<18 + t, slot>(wb0); load_w8<27 + t, slot>(wb1); }
        else if constexpr (t < 7) { load_w8<36 + t, slot>(wb0); load_w8<43 + t, slot>(wb1); }
        else {
          wvt[0][t - 7] = cat8(read_w32<slot, 0>(wb0), read_w32<slot, 1>(wb0));
          wvt[1][t - 7] = cat8(read_w32<slot, 0>(wb1), read_w32<slot, 1>(wb1));
        }
      });
  const size_t fragbase = (size_t)(g * NH + h) * NSEG * FRAGSEG;
  const unsigned* f_kv = ctl + (C_KV + g * 32 + h) * CW;
  const unsigned* f_q = ctl + (C_Q + g * 16 + (h >> 1)) * CW;
  auto prefetch0 = [&]() {
    const char* src = uniform_ptr((w == 2 ? P.kx : P.vx) + fragbase);
    const char* srq = uniform_ptr(P.qx + fragbase + j * 4096 + (w & 1) * 2048);
#pragma unroll
    for (int i = 0; i < 8; ++i) glds16s<true>(src + i * 1024, lane * 16, L_KVQ + (w - 2) * 8192 + i * 1024);
#pragma unroll
    for (int i = 0; i < 2; ++i) glds16s<true>(srq + i * 1024, lane * 16, L_KVQ + 16384 + ((w - 2) * 2 + i) * 1024);
  };
  bool have0 = false;
  auto try0 = [&]() {
    if (w >= 2 && !have0) {
      const unsigned p0 = ld_flag(f_kv), p1 = ld_flag(f_q);
      if (__builtin_amdgcn_readfirstlane(p0 < p1 ? p0 : p1) >= 1u) { prefetch0(); have0 = true; }
    }
  };
  try0();
  const unsigned wo_base = L_WO + (w < 2 ? w * 5 : 10 + (w - 2) * 4) * 2048;
  {
    const int qb = lane >> 4, q2 = (lane & 15) >> 2, p = lane & 3;
    const unsigned erow = 32 * (q2 >> 1) + 16 * (qb & 1) + 4 * (qb >> 1) + (q2 & 1);
    const unsigned dst = opaque(wo_base + lane * 16);
#pragma unroll
    for (int v = 0; v < 2; ++v) {
      const unsigned st0 = v ? ST_WO1 : ST_WO0;
      const unsigned b4 = opaque(st0 + erow * 576 + p * 8 + 4 * w * 32), b1 = opaque(st0 + erow * 576 + p * 8 + (16 + (w & 1)) * 32);
      bf16x8 lo[5], hi[5];
#pragma unroll
      for (int sl = 0; sl < 5; ++sl) {
        const unsigned a = sl < 4 ? b4 + sl * 32 : b1;
        lo[sl] = cat4(tr_read(a), tr_read(a + 2 * 576));
        hi[sl] = cat4(tr_read(a + 8 * 576), tr_read(a + 10 * 576));
      }
      SB();
#pragma unroll
      for (int sl = 0; sl < 5; ++sl)
        if (sl < 4 || w < 2) {
          lds_st<bf16x8>(dst + v * 36864 + sl * 2048, lo[sl]);
          lds_st<bf16x8>(dst + v * 36864 + sl * 2048 + 1024, hi[sl]);
        }
      if (v == 0) wg_barrier();
    }
  }
  wg_barrier();
  try0();
#pragma unroll
  for (int T = 0; T < 2; ++T)
#pragma unroll
    for (int t = 0; t < 2; ++t) {
      const unsigned a = L_WV + w * 8192 + (t * 2 + T) * 2048 + lane * 16;
      lds_st<bf16x8>(a, __builtin_shufflevector(wvt[T][t], wvt[T][t], 0, 1, 2, 3, 4, 5, 6, 7));
      lds_st<bf16x8>(a + 1024, __builtin_shufflevector(wvt[T][t], wvt[T][t], 8, 9, 10, 11, 12, 13, 14, 15));
    }
  wait_ge(ctl + C_PAR * CW, 4u << 16, ctl);
  const unsigned parw = __builtin_amdgcn_readfirstlane(ld_flag(ctl + C_PAR * CW));
  const unsigned parP = (((parw & 0xFFFFu) > 4096u ? 1u : 0u) ^ g) & 1u, parM = parP ^ 1u;
#pragma unroll
  for (int k = 0; k < 3; ++k) {
    const int d = tid + 256 * k;
    const unsigned pos = (unsigned)(((d & ~3) | ((d & 1) << 1) | ((d >> 1) & 1)) ^ (((d >> 4) & 1) << 3));
    if (d < DM)
      lds_st<u32x4>(L_MEM + pos * 16, u32x4{m0v[k][0] | ((unsigned)m0v[k][1] << 16), m0v[k][2] | ((unsigned)m0v[k][3] << 16),
                                            m0v[k][4] | ((unsigned)m0v[k][5] << 16), m0v[k][6] | ((unsigned)m0v[k][7] << 16)});
  }

  const int lr = lane >> 4, c16 = lane & 15;
  const unsigned lane16 = lane * 16;
  unsigned* f_ox = ctl + (C_OX + g * 64 + c) * CW;
  {
    const unsigned a_mem = L_MEM + lr * 256 + (lr & 1) * 128 + c16 * 8;
    const unsigned a_imgw = L_QT + (unsigned)(w * 512 + (lane >> 5) * 256 + c16 * 16 + (lr & 1) * 8);
    const unsigned a_imgr = L_QT + (unsigned)(lr * 64 + (c16 >> 2) * 16 + (lane & 1) * 8);
    const unsigned a_oir = L_OI + ((lane >> 3) & 1) * 512 + lr * 128 + (lane & 7) * 16;
    const unsigned a_oiw = L_OI + w * 512 + lr * 128 + c16 * 16;
    const unsigned gpos = L_MEM + (w == 3 ? 4096 : 0) + (unsigned)(((lane & ~3) | ((lane & 1) << 1) | ((lane >> 1) & 1)) ^ (((lane >> 4) & 1) << 3)) * 16;
    const unsigned pofs = (unsigned)(((lr == 1) ? 2 : (lr == 2) ? 1 : lr) * 16384 + (c16 >> 3) * 4096 + (c16 & 7) * 16);
    const unsigned sidx = (lane & 8) ? 0xEEEEu : 0x4444u;
    unsigned kvl = L_KVQ + lane16, kvw = L_KVQ + w * 2048 + lane16;
    const unsigned wol = wo_base + lane16, wvl = L_WV + w * 8192 + lane16;
    const unsigned slotP = lmap(72 * 4096, 0);
    const int red = (w == 0) ? c : ((w == 1 && c < 8) ? 64 + c : -1);
    const uint64_t ga = (uint64_t)(pageM + (parM << 13) + (w == 3 ? 4096 : 0));
    const uint64_t gb = (uint64_t)(pageM + (parM << 13) + 16384 + (w == 3 ? 0 : 1024));
    const uint64_t mc = (uint64_t)(pageM + (parM << 13) + 16384 + 1024 + 32768);
    const uint64_t pw = (uint64_t)(pageP + (size_t)(18 * j + 4 * w) * 16384 + (parP << 13) + h * 128);
    const uint64_t px = (uint64_t)(pageP + (size_t)(18 * j + 16 + (w & 1)) * 16384 + (parP << 13) + h * 128);
    const uint64_t pr = (uint64_t)(pageP + lmap((red < 0 ? 0 : red) * 4096, parP));
    const uint64_t mw = (uint64_t)(pageM + lmap((red < 0 ? 0 : red) * 128, parM) + 32768);
    const uint64_t oxp = (uint64_t)(P.ox + fragbase + j * 4096 + w * 1024);
    const uint64_t ds0 = (uint64_t)((w == 2 ? P.kx : P.vx) + fragbase + FRAGSEG);
    const uint64_t dsq = (uint64_t)(P.qx + fragbase + FRAGSEG + j * 4096 + (w & 1) * 2048);
    const unsigned role = (red >= 0 ? 1u : 0u) | (w >= 2 ? 2u : 0u) | (w == 3 ? 4u : 0u) | (w < 2 ? 8u : 0u) | (w == 1 ? 16u : 0u) | (w == 0 ? 32u : 0u) | (w == 0 && c == 0 ? 64u : 0u);
    const int qx = 16 * j + c16;
    uint64_t m1[4], m2[4];
#pragma unroll
    for (int r = 0; r < 4; ++r) { m1[r] = __ballot(4 * lr + r > qx); m2[r] = __ballot(16 + 4 * lr + r > qx); }
#define LO(x) (unsigned)(uint64_t)(x)
#define HI(x) (unsigned)((uint64_t)(x) >> 32)
    const uint64_t prf = 0;
    const unsigned pv[63] = {0u, SPIN_CAP, 0x10000u, role, __float_as_uint(SM_C),
                             LO(ga), HI(ga), LO(gb), HI(gb), DELAY1, DELAY2, LO(pw), HI(pw), LO(px), HI(px), LO(pr), HI(pr),
                             slotP, 0u, LO(mw), HI(mw), LO(oxp), HI(oxp), LO(ds0), HI(ds0), LO(dsq), HI(dsq),
                             L_KVQ + KVQ_SZ + (w & 1) * 8192, L_KVQ + KVQ_SZ + 16384 + (w & 1) * 2048,
                             LO(f_kv), HI(f_kv), LO(f_q), HI(f_q), LO(f_ox), HI(f_ox), LO(ctl + C_TMO * CW), HI(ctl + C_TMO * CW),
                             LO(m1[0]), HI(m1[0]), LO(m1[1]), HI(m1[1]), LO(m1[2]), HI(m1[2]), LO(m1[3]), HI(m1[3]),
                             LO(m2[0]), HI(m2[0]), LO(m2[1]), HI(m2[1]), LO(m2[2]), HI(m2[2]), LO(m2[3]), HI(m2[3]), KVQ_SZ, LO(prf), HI(prf), LO(mc), HI(mc), AHEAD1S, AHEAD2, w == 2 ? 0u : ~0u, AHEAD1, AHEAD3};
#undef LO
#undef HI
    unsigned par = 0;
    static_for<63>([&](auto ic) {
      constexpr int i = decltype(ic)::value;
      put_lane<i>(par, pv[i]);
    });
    asm volatile("" : "+v"(par));
    if (w >= 2 && !have0) {
      wait_ge(f_kv, 1u, ctl);
      wait_ge(f_q, 1u, ctl);
      prefetch0();
    }
    if (tid == 0) lds_st<u32x4>(162816, u32x4{0u, 0u, 0u, 0u});
    wait_ge(ctl + (C_INIT + g) * CW, 16u, ctl);
    vm_drain();
    asm volatile("s_waitcnt lgkmcnt(0)" ::: "memory");
    unsigned pfkv = 0;
    asm volatile(
    "s_nop 1\n\t"
    "v_readlane_b32 s40, v15, 0\n\t"
    "v_readlane_b32 s42, v15, 1\n\t"
    "v_readlane_b32 s43, v15, 2\n\t"
    "v_readlane_b32 s44, v15, 3\n\t"
    "v_readlane_b32 s98, v15, 4\n\t"
    "v_readlane_b32 s46, v15, 5\n\t"
    "v_readlane_b32 s47, v15, 6\n\t"
    "v_readlane_b32 s48, v15, 7\n\t"
    "v_readlane_b32 s49, v15, 8\n\t"
    "v_readlane_b32 s50, v15, 9\n\t"
    "v_readlane_b32 s51, v15, 10\n\t"
    "v_readlane_b32 s52, v15, 11\n\t"
    "v_readlane_b32 s53, v15, 12\n\t"
    "v_readlane_b32 s54, v15, 13\n\t"
    "v_readlane_b32 s55, v15, 14\n\t"
    "v_readlane_b32 s56, v15, 15\n\t"
    "v_readlane_b32 s57, v15, 16\n\t"
    "v_readlane_b32 s58, v15, 17\n\t"
    "v_readlane_b32 s59, v15, 18\n\t"
    "v_readlane_b32 s60, v15, 19\n\t"
    "v_readlane_b32 s61, v15, 20\n\t"
    "v_readlane_b32 s62, v15, 21\n\t"
    "v_readlane_b32 s63, v15, 22\n\t"
    "v_readlane_b32 s64, v15, 23\n\t"
    "v_readlane_b32 s65, v15, 24\n\t"
    "v_readlane_b32 s66, v15, 25\n\t"
    "v_readlane_b32 s67, v15, 26\n\t"
    "v_readlane_b32 s68, v15, 27\n\t"
    "v_readlane_b32 s69, v15, 28\n\t"
    "v_readlane_b32 s70, v15, 29\n\t"
    "v_readlane_b32 s71, v15, 30\n\t"
    "v_readlane_b32 s72, v15, 31\n\t"
    "v_readlane_b32 s73, v15, 32\n\t"
    "v_readlane_b32 s74, v15, 33\n\t"
    "v_readlane_b32 s75, v15, 34\n\t"
    "v_readlane_b32 s76, v15, 35\n\t"
    "v_readlane_b32 s77, v15, 36\n\t"
    "v_readlane_b32 s78, v15, 37\n\t"
    "v_readlane_b32 s79, v15, 38\n\t"
    "v_readlane_b32 s80, v15, 39\n\t"
    "v_readlane_b32 s81, v15, 40\n\t"
    "v_readlane_b32 s82, v15, 41\n\t"
    "v_readlane_b32 s83, v15, 42\n\t"
    "v_readlane_b32 s84, v15, 43\n\t"
    "v_readlane_b32 s85, v15, 44\n\t"
    "v_readlane_b32 s86, v15, 45\n\t"
    "v_readlane_b32 s87, v15, 46\n\t"
    "v_readlane_b32 s88, v15, 47\n\t"
    "v_readlane_b32 s89, v15, 48\n\t"
    "v_readlane_b32 s90, v15, 49\n\t"
    "v_readlane_b32 s91, v15, 50\n\t"
    "v_readlane_b32 s92, v15, 51\n\t"
    "v_readlane_b32 s93, v15, 52\n\t"
    "v_readlane_b32 s99, v15, 53\n\t"
    "v_readlane_b32 s96, v15, 54\n\t"
    "v_readlane_b32 s97, v15, 55\n\t"
    "v_readlane_b32 s36, v15, 56\n\t"
    "v_readlane_b32 s37, v15, 57\n\t"
    "v_readlane_b32 s39, v15, 58\n\t"
    "v_readlane_b32 s34, v15, 59\n\t"
    "v_readlane_b32 s35, v15, 60\n\t"
    "v_readlane_b32 s33, v15, 61\n\t"
    "v_readlane_b32 s31, v15, 62\n\t"
    "s_mov_b32 s45, 0x10000\n\t"
    "s_nop 4\n\t"
    "v_mov_b64 v[36:37], 0\n\t"
    "v_mov_b64 v[38:39], 0\n\t"
    "v_mov_b64 v[40:41], 0\n\t"
    "v_mov_b64 v[42:43], 0\n\t"
    "v_mov_b64 v[44:45], 0\n\t"
    "v_mov_b64 v[46:47], 0\n\t"
    "v_mov_b64 v[48:49], 0\n\t"
    "v_mov_b64 v[50:51], 0\n\t"
    "v_mov_b64 v[52:53], 0\n\t"
    "v_mov_b64 v[54:55], 0\n\t"
    "v_mov_b64 v[56:57], 0\n\t"
    "v_mov_b64 v[58:59], 0\n\t"
    "L_top_%=:\n\t"
    "s_bitcmp1_b32 s44, 1\n\t"
    "s_cbranch_scc0 L_b1_%=\n\t"
    "s_cmp_eq_u32 s40, 0\n\t"
    "s_cbranch_scc1 L_b1_%=\n\t"
    "v_mov_b32 v32, 162824\n\t"
    "s_mov_b32 s41, 0\n\t"
    "L_lp_%=:\n\t"
    "ds_read_b64 v[34:35], v32\n\t"
    "s_waitcnt lgkmcnt(0)\n\t"
    "v_readfirstlane_b32 s94, v34\n\t"
    "v_readfirstlane_b32 s95, v35\n\t"
    "s_cmp_eq_u32 s94, s40\n\t"
    "s_cbranch_scc1 L_lpok_%=\n\t"
    "s_sleep 1\n\t"
    "s_add_u32 s41, s41, 1\n\t"
    "s_cmp_lt_u32 s41, s42\n\t"
    "s_cbranch_scc1 L_lp_%=\n\t"
    "L_lpok_%=:\n\t"
    "s_add_u32 s95, s95, s34\n\t"
    "s_cmp_lt_u32 s40, 2\n\t"
    "s_cbranch_scc1 L_c2_%=\n\t"
    "s_add_u32 s94, s38, s31\n\t"
    "s_sub_u32 vcc_lo, s94, s95\n\t"
    "s_cmp_lt_u32 vcc_lo, 300\n\t"
    "s_cselect_b32 s95, s94, s95\n\t"
    "L_c2_%=:\n\t"
    "s_memrealtime vcc\n\t"
    "s_waitcnt lgkmcnt(0)\n\t"
    "s_sub_u32 s94, vcc_lo, s95\n\t"
    "s_add_u32 s94, s94, 2000\n\t"
    "s_cmp_lt_u32 s94, 2000\n\t"
    "s_cbranch_scc1 L_c2_%=\n\t"
    "v_and_b32 v35, s35, v0\n\t"
    "s_mov_b32 s41, 0\n\t"
    "L_gp_%=:\n\t"
    "global_load_dwordx4 v[60:63], v0, s[46:47] offset:0 sc1\n\t"
    "global_load_dwordx4 v[64:67], v0, s[46:47] offset:1024 sc1\n\t"
    "global_load_dwordx4 v[68:71], v0, s[46:47] offset:2048 sc1\n\t"
    "global_load_dwordx4 v[72:75], v0, s[46:47] offset:3072 sc1\n\t"
    "global_load_dwordx4 v[76:79], v35, s[48:49] sc1\n\t"
    "s_waitcnt vmcnt(0)\n\t"
    "v_max3_u32 v32, v61, v63, v65\n\t"
    "v_max3_u32 v33, v67, v69, v71\n\t"
    "v_max3_u32 v34, v73, v75, v77\n\t"
    "v_max3_u32 v32, v32, v33, v34\n\t"
    "v_max_u32 v32, v32, v79\n\t"
    "v_cmp_eq_u32 vcc, -1, v32\n\t"
    "s_cbranch_vccz L_gok_%=\n\t"
    "s_add_u32 s41, s41, 1\n\t"
    "s_cmp_lt_u32 s41, s42\n\t"
    "s_cbranch_scc1 L_gp_%=\n\t"
    "s_mov_b32 s42, 0\n\t"
    "v_mov_b32 v32, 1\n\t"
    "v_mov_b32 v33, 0\n\t"
    "global_store_dword v33, v32, s[76:77] sc1\n\t"
    "L_gok_%=:\n\t"
    "ds_write_b128 v7, v[60:63] offset:0\n\t"
    "ds_write_b128 v7, v[64:67] offset:1024\n\t"
    "ds_write_b128 v7, v[68:71] offset:2048\n\t"
    "ds_write_b128 v7, v[72:75] offset:3072\n\t"
    "s_bitcmp1_b32 s44, 2\n\t"
    "s_cbranch_scc0 L_gw2_0_%=\n\t"
    "ds_write_b128 v7, v[76:79] offset:4096\n\t"
    "s_branch L_gw3_0_%=\n\t"
    "L_gw2_0_%=:\n\t"
    "v_mov_b32 v80, 162816\n\t"
    "ds_write_b32 v80, v76\n\t"
    "L_gw3_0_%=:\n\t"
    "L_g3_%=:\n\t"
    "L_b1_%=:\n\t"
    "s_waitcnt vmcnt(1) lgkmcnt(0)\n\t"
    "s_barrier\n\t"
    "ds_read_b64_tr_b16 v[60:61], v1 offset:0\n\t"
    "ds_read_b64_tr_b16 v[62:63], v2 offset:0\n\t"
    "ds_read_b64_tr_b16 v[64:65], v1 offset:1024\n\t"
    "ds_read_b64_tr_b16 v[66:67], v2 offset:1024\n\t"
    "ds_read_b64_tr_b16 v[68:69], v1 offset:2048\n\t"
    "ds_read_b64_tr_b16 v[70:71], v2 offset:2048\n\t"
    "ds_read_b64_tr_b16 v[72:73], v1 offset:3072\n\t"
    "ds_read_b64_tr_b16 v[74:75], v2 offset:3072\n\t"
    "s_waitcnt lgkmcnt(6)\n\t"
    "v_smfmac_f32_16x16x64_bf16 v[36:39], v[60:63], a[0:7], v9\n\t"
    "ds_read_b64_tr_b16 v[76:77], v1 offset:4096\n\t"
    "ds_read_b64_tr_b16 v[78:79], v2 offset:4096\n\t"
    "v_smfmac_f32_16x16x64_bf16 v[40:43], v[60:63], a[72:79], v9\n\t"
    "s_waitcnt lgkmcnt(6)\n\t"
    "v_smfmac_f32_16x16x64_bf16 v[36:39], v[64:67], a[8:15], v9\n\t"
    "ds_read_b64_tr_b16 v[80:81], v1 offset:5120\n\t"
    "ds_read_b64_tr_b16 v[82:83], v2 offset:5120\n\t"
    "v_smfmac_f32_16x16x64_bf16 v[40:43], v[64:67], a[80:87], v9\n\t"
    "v_mov_b32 v32, 0\n\t"
    "s_waitcnt lgkmcnt(6)\n\t"
    "v_smfmac_f32_16x16x64_bf16 v[36:39], v[68:71], a[16:23], v9\n\t"
    "ds_read_b64_tr_b16 v[84:85], v1 offset:6144\n\t"
    "ds_read_b64_tr_b16 v[86:87], v2 offset:6144\n\t"
    "v_smfmac_f32_16x16x64_bf16 v[40:43], v[68:71], a[88:95], v9\n\t"
    "global_load_dword v13, v32, s[70:71] sc1\n\t"
    "s_waitcnt lgkmcnt(6)\n\t"
    "v_smfmac_f32_16x16x64_bf16 v[36:39], v[72:75], a[24:31], v9\n\t"
    "ds_read_b64_tr_b16 v[88:89], v1 offset:7168\n\t"
    "ds_read_b64_tr_b16 v[90:91], v2 offset:7168\n\t"
    "v_smfmac_f32_16x16x64_bf16 v[40:43], v[72:75], a[96:103], v9\n\t"
    "global_load_dword v15, v32, s[72:73] sc1\n\t"
    "s_waitcnt lgkmcnt(6)\n\t"
    "v_smfmac_f32_16x16x64_bf16 v[36:39], v[76:79], a[32:39], v9\n\t"
    "ds_read_b64_tr_b16 v[92:93], v1 offset:8192\n\t"
    "ds_read_b64_tr_b16 v[94:95], v2 offset:8192\n\t"
    "v_smfmac_f32_16x16x64_bf16 v[40:43], v[76:79], a[104:111], v9\n\t"
    "ds_read_b128 v[20:23], v14 offset:0\n\t"
    "s_waitcnt lgkmcnt(7)\n\t"
    "v_smfmac_f32_16x16x64_bf16 v[36:39], v[80:83], a[40:47], v9\n\t"
    "ds_read_b128 v[24:27], v14 offset:1024\n\t"
    "v_smfmac_f32_16x16x64_bf16 v[40:43], v[80:83], a[112:119], v9\n\t"
    "ds_read_b128 v[28:31], v14 offset:2048\n\t"
    "s_waitcnt lgkmcnt(7)\n\t"
    "v_smfmac_f32_16x16x64_bf16 v[36:39], v[84:87], a[48:55], v9\n\t"
    "ds_read_b128 v[32:35], v14 offset:3072\n\t"
    "v_smfmac_f32_16x16x64_bf16 v[40:43], v[84:87], a[120:127], v9\n\t"
    "ds_read_b128 v[96:99], v14 offset:4096\n\t"
    "s_waitcnt lgkmcnt(7)\n\t"
    "v_smfmac_f32_16x16x64_bf16 v[36:39], v[88:91], a[56:63], v9\n\t"
    "ds_read_b128 v[100:103], v14 offset:5120\n\t"
    "v_smfmac_f32_16x16x64_bf16 v[40:43], v[88:91], a[128:135], v9\n\t"
    "ds_read_b128 v[104:107], v14 offset:6144\n\t"
    "s_waitcnt lgkmcnt(7)\n\t"
    "v_smfmac_f32_16x16x64_bf16 v[36:39], v[92:95], a[64:71], v9\n\t"
    "ds_read_b128 v[108:111], v14 offset:7168\n\t"
    "v_smfmac_f32_16x16x64_bf16 v[40:43], v[92:95], a[136:143], v9\n\t"
    "v_smfmac_f32_16x16x64_bf16 v[44:47], v[60:63], a[144:151], v9\n\t"
    "v_smfmac_f32_16x16x64_bf16 v[48:51], v[60:63], a[216:223], v9\n\t"
    "v_smfmac_f32_16x16x64_bf16 v[44:47], v[64:67], a[152:159], v9\n\t"
    "v_smfmac_f32_16x16x64_bf16 v[48:51], v[64:67], a[224:231], v9\n\t"
    "v_smfmac_f32_16x16x64_bf16 v[44:47], v[68:71], a[160:167], v9\n\t"
    "v_smfmac_f32_16x16x64_bf16 v[48:51], v[68:71], a[232:239], v9\n\t"
    "v_smfmac_f32_16x16x64_bf16 v[44:47], v[72:75], a[168:175], v9\n\t"
    "v_smfmac_f32_16x16x64_bf16 v[48:51], v[72:75], a[240:247], v9\n\t"
    "v_smfmac_f32_16x16x64_bf16 v[44:47], v[76:79], a[176:183], v9\n\t"
    "v_permlane32_swap_b32 v36, v40\n\t"
    "v_smfmac_f32_16x16x64_bf16 v[48:51], v[76:79], a[248:255], v9\n\t"
    "v_permlane32_swap_b32 v37, v41\n\t"
    "v_smfmac_f32_16x16x64_bf16 v[44:47], v[80:83], a[184:191], v9\n\t"
    "v_permlane32_swap_b32 v38, v42\n\t"
    "v_permlane32_swap_b32 v39, v43\n\t"
    "v_smfmac_f32_16x16x64_bf16 v[48:51], v[80:83], v[112:119], v9\n\t"
    "v_pk_add_f32 v[36:37], v[36:37], v[40:41]\n\t"
    "v_pk_add_f32 v[38:39], v[38:39], v[42:43]\n\t"
    "v_smfmac_f32_16x16x64_bf16 v[44:47], v[84:87], a[192:199], v9\n\t"
    "v_cvt_pk_bf16_f32 v36, v36, v37\n\t"
    "v_cvt_pk_bf16_f32 v37, v38, v39\n\t"
    "v_smfmac_f32_16x16x64_bf16 v[48:51], v[84:87], v[120:127], v9\n\t"
    "ds_write_b64 v3, v[36:37]\n\t"
    "v_smfmac_f32_16x16x64_bf16 v[44:47], v[88:91], a[200:207], v9\n\t"
    "v_smfmac_f32_16x16x64_bf16 v[48:51], v[88:91], v[128:135], v9\n\t"
    "v_smfmac_f32_16x16x64_bf16 v[44:47], v[92:95], a[208:215], v9\n\t"
    "ds_read_b128 v[36:39], v10 offset:0\n\t"
    "v_smfmac_f32_16x16x64_bf16 v[48:51], v[92:95], v[136:143], v9\n\t"
    "ds_read_b128 v[40:43], v10 offset:1024\n\t"
    "v_smfmac_f32_16x16x64_bf16 v[52:55], v[60:63], v[144:151], v9\n\t"
    "v_smfmac_f32_16x16x64_bf16 v[56:59], v[60:63], v[200:207], v9\n\t"
    "s_nop 5\n\t"
    "v_permlane32_swap_b32 v44, v48\n\t"
    "v_permlane32_swap_b32 v45, v49\n\t"
    "v_permlane32_swap_b32 v46, v50\n\t"
    "v_permlane32_swap_b32 v47, v51\n\t"
    "v_smfmac_f32_16x16x64_bf16 v[52:55], v[64:67], v[152:159], v9\n\t"
    "v_pk_add_f32 v[44:45], v[44:45], v[48:49]\n\t"
    "v_pk_add_f32 v[46:47], v[46:47], v[50:51]\n\t"
    "v_cvt_pk_bf16_f32 v44, v44, v45\n\t"
    "v_cvt_pk_bf16_f32 v45, v46, v47\n\t"
    "ds_write_b64 v3, v[44:45] offset:2048\n\t"
    "v_smfmac_f32_16x16x64_bf16 v[56:59], v[64:67], v[208:215], v9\n\t"
    "ds_read_b128 v[44:47], v10 offset:2048\n\t"
    "ds_read_b128 v[48:51], v10 offset:3072\n\t"
    "v_smfmac_f32_16x16x64_bf16 v[52:55], v[68:71], v[160:167], v9\n\t"
    "v_smfmac_f32_16x16x64_bf16 v[56:59], v[68:71], v[216:223], v9\n\t"
    "v_smfmac_f32_16x16x64_bf16 v[52:55], v[72:75], v[168:175], v9\n\t"
    "v_smfmac_f32_16x16x64_bf16 v[56:59], v[72:75], v[224:231], v9\n\t"
    "s_waitcnt lgkmcnt(2)\n\t"
    "s_waitcnt vmcnt(2)\n\t"
    "s_barrier\n\t"
    "ds_read_b64_tr_b16 v[60:61], v4 offset:0\n\t"
    "ds_read_b64_tr_b16 v[62:63], v4 offset:256\n\t"
    "ds_read_b64_tr_b16 v[64:65], v4 offset:2048\n\t"
    "ds_read_b64_tr_b16 v[66:67], v4 offset:2304\n\t"
    "ds_read_b128 v[68:71], v10 offset:4096\n\t"
    "ds_read_b64_tr_b16 v[72:73], v4 offset:512\n\t"
    "ds_read_b64_tr_b16 v[74:75], v4 offset:768\n\t"
    "v_smfmac_f32_16x16x64_bf16 v[52:55], v[76:79], v[176:183], v9\n\t"
    "s_bitcmp1_b32 s44, 4\n\t"
    "s_cbranch_scc0 L_nofl_%=\n\t"
    "v_mov_b32 v16, s40\n\t"
    "v_mov_b32 v17, 0\n\t"
    "global_store_dword v17, v16, s[74:75] sc1\n\t"
    "L_nofl_%=:\n\t"
    "v_smfmac_f32_16x16x64_bf16 v[56:59], v[76:79], v[232:239], v9\n\t"
    "ds_read_b64_tr_b16 v[76:77], v4 offset:2560\n\t"
    "ds_read_b64_tr_b16 v[78:79], v4 offset:2816\n\t"
    "v_smfmac_f32_16x16x64_bf16 v[52:55], v[80:83], v[184:191], v9\n\t"
    "v_smfmac_f32_16x16x64_bf16 v[56:59], v[80:83], v[240:247], v9\n\t"
    "ds_read_b128 v[80:83], v10 offset:5120\n\t"
    "v_smfmac_f32_16x16x64_bf16 v[52:55], v[84:87], v[192:199], v9\n\t"
    "v_smfmac_f32_16x16x64_bf16 v[56:59], v[84:87], v[248:255], v9\n\t"
    "ds_read_b64_tr_b16 v[84:85], v4 offset:1024\n\t"
    "ds_read_b64_tr_b16 v[86:87], v4 offset:1280\n\t"
    "v_smfmac_f32_16x16x64_bf16 v[52:55], v[88:91], v[20:27], v9\n\t"
    "v_smfmac_f32_16x16x64_bf16 v[56:59], v[88:91], v[28:35], v9\n\t"
    "ds_read_b64_tr_b16 v[88:89], v4 offset:3072\n\t"
    "ds_read_b64_tr_b16 v[90:91], v4 offset:3328\n\t"
    "ds_read_b128 v[20:23], v10 offset:6144\n\t"
    "ds_read_b64_tr_b16 v[24:25], v4 offset:1536\n\t"
    "ds_read_b64_tr_b16 v[26:27], v4 offset:1792\n\t"
    "ds_read_b64_tr_b16 v[28:29], v4 offset:3584\n\t"
    "ds_read_b64_tr_b16 v[30:31], v4 offset:3840\n\t"
    "ds_read_b128 v[32:35], v10 offset:7168\n\t"
    "v_smfmac_f32_16x16x64_bf16 v[52:55], v[92:95], v[96:103], v9\n\t"
    "v_smfmac_f32_16x16x64_bf16 v[56:59], v[92:95], v[104:111], v9\n\t"
    "s_cmp_eq_u32 s40, 63\n\t"
    "s_cbranch_scc0 L_notlast_%=\n\t"
    "s_nop 7\n\t"
    "v_cvt_pk_bf16_f32 v16, v52, v53\n\t"
    "v_cvt_pk_bf16_f32 v17, v54, v55\n\t"
    "v_cvt_pk_bf16_f32 v18, v56, v57\n\t"
    "v_cvt_pk_bf16_f32 v19, v58, v59\n\t"
    "s_branch L_xat_%=\n\t"
    "L_notlast_%=:\n\t"
    "s_waitcnt lgkmcnt(15)\n\t"
    "v_mfma_f32_16x16x32_bf16 v[96:99], v[36:39], v[60:63], 0\n\t"
    "v_mfma_f32_16x16x32_bf16 v[100:103], v[68:71], v[60:63], 0\n\t"
    "v_mfma_f32_16x16x32_bf16 v[104:107], v[64:67], v[60:63], 0\n\t"
    "s_waitcnt lgkmcnt(10)\n\t"
    "v_mfma_f32_16x16x32_bf16 v[96:99], v[40:43], v[72:75], v[96:99]\n\t"
    "v_mfma_f32_16x16x32_bf16 v[100:103], v[80:83], v[72:75], v[100:103]\n\t"
    "v_mfma_f32_16x16x32_bf16 v[104:107], v[76:79], v[72:75], v[104:107]\n\t"
    "s_waitcnt lgkmcnt(5)\n\t"
    "v_mfma_f32_16x16x32_bf16 v[96:99], v[44:47], v[84:87], v[96:99]\n\t"
    "v_mfma_f32_16x16x32_bf16 v[100:103], v[20:23], v[84:87], v[100:103]\n\t"
    "v_mfma_f32_16x16x32_bf16 v[104:107], v[88:91], v[84:87], v[104:107]\n\t"
    "s_waitcnt lgkmcnt(0)\n\t"
    "v_mfma_f32_16x16x32_bf16 v[96:99], v[48:51], v[24:27], v[96:99]\n\t"
    "v_mfma_f32_16x16x32_bf16 v[100:103], v[32:35], v[24:27], v[100:103]\n\t"
    "v_mfma_f32_16x16x32_bf16 v[104:107], v[28:31], v[24:27], v[104:107]\n\t"
    "v_cvt_pk_bf16_f32 v16, v52, v53\n\t"
    "v_cvt_pk_bf16_f32 v17, v54, v55\n\t"
    "v_cvt_pk_bf16_f32 v18, v56, v57\n\t"
    "v_cvt_pk_bf16_f32 v19, v58, v59\n\t"
    "ds_read_b128 v[92:95], v11 offset:8192\n\t"
    "ds_read_b128 v[108:111], v11 offset:9216\n\t"
    "ds_read_b128 v[60:63], v12 offset:8192\n\t"
    "ds_read_b128 v[64:67], v12 offset:9216\n\t"
    "v_max3_f32 v20, v96, v97, v98\n\t"
    "v_max3_f32 v21, v99, v100, v101\n\t"
    "v_max3_f32 v22, v102, v103, v104\n\t"
    "v_max3_f32 v23, v105, v106, v107\n\t"
    "v_max3_f32 v20, v20, v21, v22\n\t"
    "v_max_f32 v20, v20, v23\n\t"
    "v_mov_b32 v21, v20\n\t"
    "ds_read_b128 v[68:71], v12 offset:45056\n\t"
    "ds_read_b128 v[72:75], v12 offset:46080\n\t"
    "v_permlane16_swap_b32 v20, v21\n\t"
    "v_max_f32 v20, v20, v21\n\t"
    "v_mov_b32 v21, v20\n\t"
    "ds_read_b128 v[76:79], v12 offset:0\n\t"
    "ds_read_b128 v[80:83], v12 offset:1024\n\t"
    "v_permlane32_swap_b32 v20, v21\n\t"
    "v_max_f32 v20, v20, v21\n\t"
    "v_mul_f32 v22, s98, v20\n\t"
    "v_pk_fma_f32 v[104:105], v[104:105], s[98:99], v[22:23] op_sel_hi:[1,0,0] neg_lo:[0,0,1] neg_hi:[0,0,1]\n\t"
    "v_pk_fma_f32 v[106:107], v[106:107], s[98:99], v[22:23] op_sel_hi:[1,0,0] neg_lo:[0,0,1] neg_hi:[0,0,1]\n\t"
    "v_pk_fma_f32 v[96:97], v[96:97], s[98:99], v[22:23] op_sel_hi:[1,0,0] neg_lo:[0,0,1] neg_hi:[0,0,1]\n\t"
    "v_exp_f32 v104, v104\n\t"
    "v_exp_f32 v105, v105\n\t"
    "v_pk_fma_f32 v[98:99], v[98:99], s[98:99], v[22:23] op_sel_hi:[1,0,0] neg_lo:[0,0,1] neg_hi:[0,0,1]\n\t"
    "v_exp_f32 v106, v106\n\t"
    "v_exp_f32 v107, v107\n\t"
    "v_pk_fma_f32 v[100:101], v[100:101], s[98:99], v[22:23] op_sel_hi:[1,0,0] neg_lo:[0,0,1] neg_hi:[0,0,1]\n\t"
    "v_exp_f32 v96, v96\n\t"
    "v_exp_f32 v97, v97\n\t"
    "v_cvt_pk_bf16_f32 v24, v104, v105\n\t"
    "v_cvt_pk_bf16_f32 v25, v106, v107\n\t"
    "v_pk_fma_f32 v[102:103], v[102:103], s[98:99], v[22:23] op_sel_hi:[1,0,0] neg_lo:[0,0,1] neg_hi:[0,0,1]\n\t"
    "v_exp_f32 v98, v98\n\t"
    "v_exp_f32 v99, v99\n\t"
    "v_mfma_f32_16x16x16_bf16 v[52:55], v[16:17], v[24:25], 0\n\t"
    "v_mfma_f32_16x16x16_bf16 v[56:59], v[18:19], v[24:25], 0\n\t"
    "v_exp_f32 v100, v100\n\t"
    "v_exp_f32 v101, v101\n\t"
    "v_exp_f32 v102, v102\n\t"
    "v_exp_f32 v103, v103\n\t"
    "v_cvt_pk_bf16_f32 v28, v96, v97\n\t"
    "v_cvt_pk_bf16_f32 v29, v98, v99\n\t"
    "v_cvt_pk_bf16_f32 v30, v100, v101\n\t"
    "v_cvt_pk_bf16_f32 v31, v102, v103\n\t"
    "v_pk_add_f32 v[26:27], v[104:105], v[106:107]\n\t"
    "v_pk_add_f32 v[32:33], v[96:97], v[98:99]\n\t"
    "v_pk_add_f32 v[34:35], v[100:101], v[102:103]\n\t"
    "s_waitcnt lgkmcnt(6)\n\t"
    "v_mfma_f32_16x16x32_bf16 v[52:55], v[92:95], v[28:31], v[52:55]\n\t"
    "v_mfma_f32_16x16x32_bf16 v[56:59], v[108:111], v[28:31], v[56:59]\n\t"
    "ds_read_b128 v[84:87], v12 offset:36864\n\t"
    "ds_read_b128 v[88:91], v12 offset:37888\n\t"
    "ds_read_b128 v[36:39], v12 offset:2048\n\t"
    "ds_read_b128 v[40:43], v12 offset:3072\n\t"
    "ds_read_b128 v[44:47], v12 offset:38912\n\t"
    "ds_read_b128 v[48:51], v12 offset:39936\n\t"
    "v_pk_add_f32 v[32:33], v[32:33], v[34:35]\n\t"
    "v_add_f32 v26, v26, v27\n\t"
    "v_add_f32 v32, v32, v33\n\t"
    "v_fmac_f32 v32, 0.5, v26\n\t"
    "v_mov_b32 v33, v32\n\t"
    "s_nop 1\n\t"
    "v_permlane16_swap_b32 v32, v33\n\t"
    "v_add_f32 v32, v32, v33\n\t"
    "v_mov_b32 v33, v32\n\t"
    "s_nop 1\n\t"
    "v_permlane32_swap_b32 v32, v33\n\t"
    "v_add_f32 v32, v32, v33\n\t"
    "v_rcp_f32 v22, v32\n\t"
    "s_nop 0\n\t"
    "v_pk_mul_f32 v[52:53], v[52:53], v[22:23] op_sel_hi:[1,0]\n\t"
    "v_pk_mul_f32 v[54:55], v[54:55], v[22:23] op_sel_hi:[1,0]\n\t"
    "v_pk_mul_f32 v[56:57], v[56:57], v[22:23] op_sel_hi:[1,0]\n\t"
    "v_pk_mul_f32 v[58:59], v[58:59], v[22:23] op_sel_hi:[1,0]\n\t"
    "v_cvt_pk_bf16_f32 v52, v52, v53\n\t"
    "v_cvt_pk_bf16_f32 v53, v54, v55\n\t"
    "v_cvt_pk_bf16_f32 v54, v56, v57\n\t"
    "v_cvt_pk_bf16_f32 v55, v58, v59\n\t"
    "s_mov_b32 exec_lo, 0x00ff00ff\n\t"
    "s_mov_b32 exec_hi, 0x00ff00ff\n\t"
    "ds_write_b128 v6, v[52:55]\n\t"
    "s_mov_b64 exec, -1\n\t"
    "ds_read_b128 v[20:23], v12 offset:4096\n\t"
    "ds_read_b128 v[24:27], v12 offset:5120\n\t"
    "ds_read_b128 v[28:31], v12 offset:40960\n\t"
    "ds_read_b128 v[32:35], v12 offset:41984\n\t"
    "v_mov_b64 v[96:97], 0\n\t"
    "v_mov_b64 v[98:99], 0\n\t"
    "v_mov_b64 v[100:101], 0\n\t"
    "v_mov_b64 v[102:103], 0\n\t"
    "v_mov_b64 v[104:105], 0\n\t"
    "v_mov_b64 v[106:107], 0\n\t"
    "v_mov_b64 v[108:109], 0\n\t"
    "v_mov_b64 v[110:111], 0\n\t"
    "v_mov_b64 v[92:93], 0\n\t"
    "v_mov_b64 v[94:95], 0\n\t"
    "s_waitcnt lgkmcnt(4)\n\t"
    "s_barrier\n\t"
    "ds_read_b128 v[52:55], v5 offset:0\n\t"
    "ds_read_b128 v[56:59], v5 offset:1024\n\t"
    "s_bitcmp1_b32 s44, 3\n\t"
    "s_cbranch_scc0 L_dy_%=\n\t"
    "s_waitcnt lgkmcnt(0)\n\t"
    "v_smfmac_f32_16x16x64_bf16 v[92:95], v[52:55], v[60:67], v9\n\t"
    "v_smfmac_f32_16x16x64_bf16 v[92:95], v[56:59], v[68:75], v9\n\t"
    "ds_read_b128 v[60:63], v12 offset:6144\n\t"
    "ds_read_b128 v[64:67], v12 offset:7168\n\t"
    "ds_read_b128 v[68:71], v12 offset:43008\n\t"
    "ds_read_b128 v[72:75], v12 offset:44032\n\t"
    "v_smfmac_f32_16x16x64_bf16 v[96:99], v[52:55], v[76:83], v9\n\t"
    "v_smfmac_f32_16x16x64_bf16 v[96:99], v[56:59], v[84:91], v9\n\t"
    "v_smfmac_f32_16x16x64_bf16 v[100:103], v[52:55], v[36:43], v9\n\t"
    "v_smfmac_f32_16x16x64_bf16 v[100:103], v[56:59], v[44:51], v9\n\t"
    "v_mov_b64 v[76:77], v[92:93]\n\t"
    "v_mov_b64 v[78:79], v[94:95]\n\t"
    "v_smfmac_f32_16x16x64_bf16 v[104:107], v[52:55], v[20:27], v9\n\t"
    "v_mov_b32 v80, s43\n\t"
    "v_permlane32_swap_b32 v92, v76\n\t"
    "v_permlane32_swap_b32 v93, v77\n\t"
    "v_permlane32_swap_b32 v94, v78\n\t"
    "v_permlane32_swap_b32 v95, v79\n\t"
    "v_smfmac_f32_16x16x64_bf16 v[104:107], v[56:59], v[28:35], v9\n\t"
    "v_pk_add_f32 v[92:93], v[92:93], v[76:77]\n\t"
    "v_pk_add_f32 v[94:95], v[94:95], v[78:79]\n\t"
    "v_cvt_pk_bf16_f32 v92, v92, v93\n\t"
    "v_cvt_pk_bf16_f32 v93, v94, v95\n\t"
    "v_mov_b64 v[94:95], v[92:93]\n\t"
    "s_waitcnt lgkmcnt(0)\n\t"
    "v_smfmac_f32_16x16x64_bf16 v[108:111], v[52:55], v[60:67], v9\n\t"
    "v_permlane16_swap_b32 v92, v94\n\t"
    "v_permlane16_swap_b32 v93, v95\n\t"
    "v_bfi_b32 v93, s45, v80, v93\n\t"
    "v_bfi_b32 v95, s45, v80, v95\n\t"
    "s_mov_b64 exec, 0xffff\n\t"
    "global_store_dwordx4 v8, v[92:95], s[54:55] sc1\n\t"
    "s_mov_b64 exec, -1\n\t"
    "v_smfmac_f32_16x16x64_bf16 v[108:111], v[56:59], v[68:75], v9\n\t"
    "v_permlane32_swap_b32 v96, v100\n\t"
    "v_permlane32_swap_b32 v97, v101\n\t"
    "v_permlane32_swap_b32 v98, v102\n\t"
    "v_permlane32_swap_b32 v99, v103\n\t"
    "v_pk_add_f32 v[96:97], v[96:97], v[100:101]\n\t"
    "v_pk_add_f32 v[98:99], v[98:99], v[102:103]\n\t"
    "v_cvt_pk_bf16_f32 v96, v96, v97\n\t"
    "v_cvt_pk_bf16_f32 v97, v98, v99\n\t"
    "v_permlane32_swap_b32 v104, v108\n\t"
    "v_permlane32_swap_b32 v105, v109\n\t"
    "v_permlane32_swap_b32 v106, v110\n\t"
    "v_permlane32_swap_b32 v107, v111\n\t"
    "v_pk_add_f32 v[104:105], v[104:105], v[108:109]\n\t"
    "v_pk_add_f32 v[106:107], v[106:107], v[110:111]\n\t"
    "v_cvt_pk_bf16_f32 v98, v104, v105\n\t"
    "v_cvt_pk_bf16_f32 v99, v106, v107\n\t"
    "s_nop 1\n\t"
    "v_permlane16_swap_b32 v96, v98\n\t"
    "v_permlane16_swap_b32 v97, v99\n\t"
    "v_bfi_b32 v97, s45, v80, v97\n\t"
    "v_bfi_b32 v99, s45, v80, v99\n\t"
    "global_store_dwordx4 v8, v[96:99], s[52:53] sc1\n\t"
    "s_branch L_nox_%=\n\t"
    "L_dy_%=:\n\t"
    "ds_read_b128 v[60:63], v12 offset:6144\n\t"
    "ds_read_b128 v[64:67], v12 offset:7168\n\t"
    "ds_read_b128 v[68:71], v12 offset:43008\n\t"
    "ds_read_b128 v[72:75], v12 offset:44032\n\t"
    "s_waitcnt lgkmcnt(4)\n\t"
    "v_smfmac_f32_16x16x64_bf16 v[96:99], v[52:55], v[76:83], v9\n\t"
    "v_smfmac_f32_16x16x64_bf16 v[96:99], v[56:59], v[84:91], v9\n\t"
    "v_smfmac_f32_16x16x64_bf16 v[100:103], v[52:55], v[36:43], v9\n\t"
    "v_smfmac_f32_16x16x64_bf16 v[100:103], v[56:59], v[44:51], v9\n\t"
    "v_smfmac_f32_16x16x64_bf16 v[104:107], v[52:55], v[20:27], v9\n\t"
    "v_smfmac_f32_16x16x64_bf16 v[104:107], v[56:59], v[28:35], v9\n\t"
    "s_waitcnt lgkmcnt(0)\n\t"
    "v_mov_b32 v80, s43\n\t"
    "v_smfmac_f32_16x16x64_bf16 v[108:111], v[52:55], v[60:67], v9\n\t"
    "v_smfmac_f32_16x16x64_bf16 v[108:111], v[56:59], v[68:75], v9\n\t"
    "s_nop 1\n\t"
    "v_permlane32_swap_b32 v96, v100\n\t"
    "v_permlane32_swap_b32 v97, v101\n\t"
    "v_permlane32_swap_b32 v98, v102\n\t"
    "v_permlane32_swap_b32 v99, v103\n\t"
    "v_pk_add_f32 v[96:97], v[96:97], v[100:101]\n\t"
    "v_pk_add_f32 v[98:99], v[98:99], v[102:103]\n\t"
    "v_cvt_pk_bf16_f32 v96, v96, v97\n\t"
    "v_cvt_pk_bf16_f32 v97, v98, v99\n\t"
    "v_permlane32_swap_b32 v104, v108\n\t"
    "v_permlane32_swap_b32 v105, v109\n\t"
    "v_permlane32_swap_b32 v106, v110\n\t"
    "v_permlane32_swap_b32 v107, v111\n\t"
    "v_pk_add_f32 v[104:105], v[104:105], v[108:109]\n\t"
    "v_pk_add_f32 v[106:107], v[106:107], v[110:111]\n\t"
    "v_cvt_pk_bf16_f32 v98, v104, v105\n\t"
    "v_cvt_pk_bf16_f32 v99, v106, v107\n\t"
    "s_nop 1\n\t"
    "v_permlane16_swap_b32 v96, v98\n\t"
    "v_permlane16_swap_b32 v97, v99\n\t"
    "v_bfi_b32 v97, s45, v80, v97\n\t"
    "v_bfi_b32 v99, s45, v80, v99\n\t"
    "global_store_dwordx4 v8, v[96:99], s[52:53] sc1\n\t"
    "L_nox_%=:\n\t"
    "L_nopub_%=:\n\t"
    "s_memtime s[100:101]\n\t"
    "v_mov_b32 v37, 162816\n\t"
    "ds_read_b32 v38, v37\n\t"
    "s_waitcnt lgkmcnt(0)\n\t"
    "v_readfirstlane_b32 s38, v38\n\t"
    "s_add_u32 s38, s38, s39\n\t"
    "s_bitcmp1_b32 s44, 1\n\t"
    "s_cbranch_scc0 L_nodma_%=\n\t"
    "s_waitcnt vmcnt(1)\n\t"
    "s_add_u32 s94, s40, 2\n\t"
    "v_readfirstlane_b32 s95, v13\n\t"
    "v_readfirstlane_b32 vcc_lo, v15\n\t"
    "s_min_u32 s95, s95, vcc_lo\n\t"
    "s_cmp_ge_u32 s95, s94\n\t"
    "s_cbranch_scc1 L_dma_%=\n\t"
    "s_mov_b32 s41, 0\n\t"
    "L_pfp_%=:\n\t"
    "v_mov_b32 v32, 0\n\t"
    "global_load_dword v13, v32, s[70:71] sc1\n\t"
    "global_load_dword v15, v32, s[72:73] sc1\n\t"
    "s_waitcnt vmcnt(0)\n\t"
    "v_readfirstlane_b32 s95, v13\n\t"
    "v_readfirstlane_b32 vcc_lo, v15\n\t"
    "s_min_u32 s95, s95, vcc_lo\n\t"
    "s_cmp_ge_u32 s95, s94\n\t"
    "s_cbranch_scc1 L_dma_%=\n\t"
    "s_sleep 2\n\t"
    "s_add_u32 s41, s41, 1\n\t"
    "s_cmp_lt_u32 s41, s42\n\t"
    "s_cbranch_scc1 L_pfp_%=\n\t"
    "s_mov_b32 s42, 0\n\t"
    "v_mov_b32 v32, 1\n\t"
    "v_mov_b32 v33, 0\n\t"
    "global_store_dword v33, v32, s[76:77] sc1\n\t"
    "L_dma_%=:\n\t"
    "s_mov_b32 m0, s68\n\t"
    "s_add_u32 vcc_lo, s64, 4096\n\t"
    "s_addc_u32 vcc_hi, s65, 0\n\t"
    "global_load_lds_dwordx4 v0, s[64:65] offset:0 sc1\n\t"
    "global_load_lds_dwordx4 v0, s[64:65] offset:1024 sc1\n\t"
    "global_load_lds_dwordx4 v0, s[64:65] offset:2048 sc1\n\t"
    "global_load_lds_dwordx4 v0, s[64:65] offset:3072 sc1\n\t"
    "s_add_u32 m0, s68, 4096\n\t"
    "s_nop 0\n\t"
    "global_load_lds_dwordx4 v0, vcc offset:0 sc1\n\t"
    "global_load_lds_dwordx4 v0, vcc offset:1024 sc1\n\t"
    "global_load_lds_dwordx4 v0, vcc offset:2048 sc1\n\t"
    "global_load_lds_dwordx4 v0, vcc offset:3072 sc1\n\t"
    "s_mov_b32 m0, s69\n\t"
    "s_nop 0\n\t"
    "global_load_lds_dwordx4 v0, s[66:67] offset:0 sc1\n\t"
    "global_load_lds_dwordx4 v0, s[66:67] offset:1024 sc1\n\t"
    "s_branch L_xat_%=\n\t"
    "L_nodma_%=:\n\t"
    "s_bitcmp1_b32 s44, 0\n\t"
    "s_cbranch_scc0 L_nored_%=\n\t"
    "v_lshrrev_b32 v68, 4, v0\n\t"
    "v_and_b32 v68, 15, v68\n\t"
    "v_lshrrev_b32 v69, 1, v68\n\t"
    "v_and_b32 v70, 1, v68\n\t"
    "v_lshlrev_b32 v70, 4, v70\n\t"
    "v_mov_b32 v71, 0x3f80\n\t"
    "v_lshlrev_b32 v70, v70, v71\n\t"
    "v_cmp_eq_u32 vcc, 0, v69\n\t"
    "s_nop 1\n\t"
    "v_cndmask_b32 v72, 0, v70, vcc\n\t"
    "v_cmp_eq_u32 vcc, 1, v69\n\t"
    "s_nop 1\n\t"
    "v_cndmask_b32 v73, 0, v70, vcc\n\t"
    "v_cmp_eq_u32 vcc, 2, v69\n\t"
    "s_nop 1\n\t"
    "v_cndmask_b32 v74, 0, v70, vcc\n\t"
    "v_cmp_eq_u32 vcc, 3, v69\n\t"
    "s_nop 1\n\t"
    "v_cndmask_b32 v75, 0, v70, vcc\n\t"
    "s_waitcnt lgkmcnt(0)\n\t"
    "L_w1_%=:\n\t"
    "s_memtime vcc\n\t"
    "s_waitcnt lgkmcnt(0)\n\t"
    "s_sub_u32 s94, vcc_lo, s100\n\t"
    "s_cmp_lt_u32 s94, s50\n\t"
    "s_cbranch_scc1 L_w1_%=\n\t"
    "s_cmp_lt_u32 s40, 1\n\t"
    "s_cbranch_scc1 L_c1x_%=\n\t"
    "s_mov_b32 s95, s38\n\t"
    "L_c1_%=:\n\t"
    "s_memrealtime vcc\n\t"
    "s_waitcnt lgkmcnt(0)\n\t"
    "s_sub_u32 s94, vcc_lo, s95\n\t"
    "s_add_u32 s94, s94, 2000\n\t"
    "s_cmp_lt_u32 s94, 2000\n\t"
    "s_cbranch_scc1 L_c1_%=\n\t"
    "L_c1x_%=:\n\t"
    "s_mov_b32 s41, 0\n\t"
    "global_load_dwordx4 v[76:79], v0, s[56:57] offset:0 sc1\n\t"
    "global_load_dwordx4 v[80:83], v0, s[56:57] offset:1024 sc1\n\t"
    "global_load_dwordx4 v[84:87], v0, s[56:57] offset:2048 sc1\n\t"
    "global_load_dwordx4 v[88:91], v0, s[56:57] offset:3072 sc1\n\t"
    "s_sleep 6\n\t"
    "global_load_dwordx4 v[96:99], v0, s[56:57] offset:0 sc1\n\t"
    "global_load_dwordx4 v[100:103], v0, s[56:57] offset:1024 sc1\n\t"
    "global_load_dwordx4 v[104:107], v0, s[56:57] offset:2048 sc1\n\t"
    "global_load_dwordx4 v[108:111], v0, s[56:57] offset:3072 sc1\n\t"
    "s_waitcnt vmcnt(4)\n\t"
    "v_mfma_f32_16x16x32_bf16 v[60:63], v[72:75], v[76:79], 0\n\t"
    "v_mfma_f32_16x16x32_bf16 v[60:63], v[72:75], v[80:83], v[60:63]\n\t"
    "v_mfma_f32_16x16x32_bf16 v[60:63], v[72:75], v[84:87], v[60:63]\n\t"
    "v_mfma_f32_16x16x32_bf16 v[60:63], v[72:75], v[88:91], v[60:63]\n\t"
    "v_bitop3_b32 v64, v77, v79, s43 bitop3:0x7e\n\t"
    "v_bitop3_b32 v65, v81, v83, s43 bitop3:0x7e\n\t"
    "v_bitop3_b32 v66, v85, v87, s43 bitop3:0x7e\n\t"
    "v_bitop3_b32 v67, v89, v91, s43 bitop3:0x7e\n\t"
    "v_or3_b32 v92, v64, v65, v66\n\t"
    "v_or_b32 v92, v92, v67\n\t"
    "v_and_b32 v92, s45, v92\n\t"
    "v_cmp_ne_u32 vcc, 0, v92\n\t"
    "s_cbranch_vccz L_rok_%=\n\t"
    "s_waitcnt vmcnt(0)\n\t"
    "v_mfma_f32_16x16x32_bf16 v[60:63], v[72:75], v[96:99], 0\n\t"
    "v_mfma_f32_16x16x32_bf16 v[60:63], v[72:75], v[100:103], v[60:63]\n\t"
    "v_mfma_f32_16x16x32_bf16 v[60:63], v[72:75], v[104:107], v[60:63]\n\t"
    "v_mfma_f32_16x16x32_bf16 v[60:63], v[72:75], v[108:111], v[60:63]\n\t"
    "v_bitop3_b32 v64, v97, v99, s43 bitop3:0x7e\n\t"
    "v_bitop3_b32 v65, v101, v103, s43 bitop3:0x7e\n\t"
    "v_bitop3_b32 v66, v105, v107, s43 bitop3:0x7e\n\t"
    "v_bitop3_b32 v67, v109, v111, s43 bitop3:0x7e\n\t"
    "v_or3_b32 v92, v64, v65, v66\n\t"
    "v_or_b32 v92, v92, v67\n\t"
    "v_and_b32 v92, s45, v92\n\t"
    "v_cmp_ne_u32 vcc, 0, v92\n\t"
    "s_cbranch_vccz L_rok_%=\n\t"
    "L_rp_%=:\n\t"
    "global_load_dwordx4 v[76:79], v0, s[56:57] offset:0 sc1\n\t"
    "global_load_dwordx4 v[80:83], v0, s[56:57] offset:1024 sc1\n\t"
    "global_load_dwordx4 v[84:87], v0, s[56:57] offset:2048 sc1\n\t"
    "global_load_dwordx4 v[88:91], v0, s[56:57] offset:3072 sc1\n\t"
    "s_waitcnt vmcnt(0)\n\t"
    "v_mfma_f32_16x16x32_bf16 v[60:63], v[72:75], v[76:79], 0\n\t"
    "v_mfma_f32_16x16x32_bf16 v[60:63], v[72:75], v[80:83], v[60:63]\n\t"
    "v_mfma_f32_16x16x32_bf16 v[60:63], v[72:75], v[84:87], v[60:63]\n\t"
    "v_mfma_f32_16x16x32_bf16 v[60:63], v[72:75], v[88:91], v[60:63]\n\t"
    "v_bitop3_b32 v64, v77, v79, s43 bitop3:0x7e\n\t"
    "v_bitop3_b32 v65, v81, v83, s43 bitop3:0x7e\n\t"
    "v_bitop3_b32 v66, v85, v87, s43 bitop3:0x7e\n\t"
    "v_bitop3_b32 v67, v89, v91, s43 bitop3:0x7e\n\t"
    "v_or3_b32 v92, v64, v65, v66\n\t"
    "v_or_b32 v92, v92, v67\n\t"
    "v_and_b32 v92, s45, v92\n\t"
    "v_cmp_ne_u32 vcc, 0, v92\n\t"
    "s_cbranch_vccz L_rok_%=\n\t"
    "s_add_u32 s41, s41, 1\n\t"
    "s_cmp_lt_u32 s41, s42\n\t"
    "s_cbranch_scc1 L_rp_%=\n\t"
    "s_mov_b32 s42, 0\n\t"
    "v_mov_b32 v92, 1\n\t"
    "v_mov_b32 v93, 0\n\t"
    "global_store_dword v93, v92, s[76:77] sc1\n\t"
    "L_rok_%=:\n\t"
    "v_add_f32_dpp v60, v60, v60 row_ror:8 row_mask:0xf bank_mask:0xf\n\t"
    "v_add_f32_dpp v61, v61, v61 row_ror:8 row_mask:0xf bank_mask:0xf\n\t"
    "v_add_f32_dpp v62, v62, v62 row_ror:8 row_mask:0xf bank_mask:0xf\n\t"
    "v_add_f32_dpp v63, v63, v63 row_ror:8 row_mask:0xf bank_mask:0xf\n\t"
    "v_cvt_pk_bf16_f32 v60, v60, v61\n\t"
    "v_cvt_pk_bf16_f32 v61, v62, v63\n\t"
    "v_mov_b64 v[62:63], v[60:61]\n\t"
    "s_nop 1\n\t"
    "v_permlane16_swap_b32 v60, v62\n\t"
    "v_permlane16_swap_b32 v61, v63\n\t"
    "v_min_u32 v61, -2, v61\n\t"
    "v_min_u32 v63, -2, v63\n\t"
    "s_mov_b64 exec, 0xff\n\t"
    "global_store_dwordx4 v0, v[60:63], s[60:61] sc1\n\t"
    "s_bitcmp1_b32 s44, 5\n\t"
    "s_cbranch_scc0 L_noctl_%=\n\t"
    "s_memrealtime vcc\n\t"
    "s_mov_b64 exec, 1\n\t"
    "s_add_u32 s94, s40, 1\n\t"
    "v_mov_b32 v68, s94\n\t"
    "v_mov_b32 v70, 162824\n\t"
    "s_waitcnt lgkmcnt(0)\n\t"
    "v_mov_b32 v69, vcc_lo\n\t"
    "ds_write_b64 v70, v[68:69]\n\t"
    "s_bitcmp1_b32 s44, 6\n\t"
    "s_cbranch_scc0 L_noctl_%=\n\t"
    "v_min_u32 v64, -2, v69\n\t"
    "v_mov_b32 v65, v64\n\t"
    "v_mov_b32 v66, v64\n\t"
    "v_mov_b32 v67, v64\n\t"
    "global_store_dwordx4 v0, v[64:67], s[36:37] sc1\n\t"
    "L_noctl_%=:\n\t"
    "s_mov_b64 exec, -1\n\t"
    "L_nored_%=:\n\t"
    "L_xat_%=:\n\t"
    "s_waitcnt lgkmcnt(0)\n\t"
    "ds_read_b128 v[20:23], v10 offset:16384\n\t"
    "ds_read_b128 v[24:27], v10 offset:0\n\t"
    "ds_read_b128 v[28:31], v10 offset:4096\n\t"
    "ds_read_b64_tr_b16 v[32:33], v4 offset:2048\n\t"
    "ds_read_b64_tr_b16 v[34:35], v4 offset:2304\n\t"
    "ds_read_b128 v[36:39], v10 offset:17408\n\t"
    "ds_read_b128 v[40:43], v10 offset:1024\n\t"
    "ds_read_b128 v[44:47], v10 offset:5120\n\t"
    "ds_read_b64_tr_b16 v[48:49], v4 offset:2560\n\t"
    "ds_read_b64_tr_b16 v[50:51], v4 offset:2816\n\t"
    "ds_read_b128 v[52:55], v10 offset:18432\n\t"
    "ds_read_b128 v[56:59], v10 offset:2048\n\t"
    "ds_read_b128 v[60:63], v10 offset:6144\n\t"
    "ds_read_b64_tr_b16 v[64:65], v4 offset:3072\n\t"
    "ds_read_b64_tr_b16 v[66:67], v4 offset:3328\n\t"
    "ds_read_b128 v[68:71], v10 offset:19456\n\t"
    "ds_read_b128 v[72:75], v10 offset:3072\n\t"
    "ds_read_b128 v[76:79], v10 offset:7168\n\t"
    "ds_read_b64_tr_b16 v[80:81], v4 offset:3584\n\t"
    "ds_read_b64_tr_b16 v[82:83], v4 offset:3840\n\t"
    "s_waitcnt lgkmcnt(15)\n\t"
    "v_mfma_f32_16x16x32_bf16 v[84:87], v[24:27], v[20:23], 0\n\t"
    "v_mfma_f32_16x16x32_bf16 v[88:91], v[28:31], v[20:23], 0\n\t"
    "v_mfma_f32_16x16x32_bf16 v[92:95], v[32:35], v[20:23], 0\n\t"
    "s_waitcnt lgkmcnt(10)\n\t"
    "v_mfma_f32_16x16x32_bf16 v[84:87], v[40:43], v[36:39], v[84:87]\n\t"
    "v_mfma_f32_16x16x32_bf16 v[88:91], v[44:47], v[36:39], v[88:91]\n\t"
    "v_mfma_f32_16x16x32_bf16 v[92:95], v[48:51], v[36:39], v[92:95]\n\t"
    "s_waitcnt lgkmcnt(5)\n\t"
    "v_mfma_f32_16x16x32_bf16 v[84:87], v[56:59], v[52:55], v[84:87]\n\t"
    "v_mfma_f32_16x16x32_bf16 v[88:91], v[60:63], v[52:55], v[88:91]\n\t"
    "v_mfma_f32_16x16x32_bf16 v[92:95], v[64:67], v[52:55], v[92:95]\n\t"
    "s_waitcnt lgkmcnt(0)\n\t"
    "v_mfma_f32_16x16x32_bf16 v[84:87], v[72:75], v[68:71], v[84:87]\n\t"
    "v_mfma_f32_16x16x32_bf16 v[88:91], v[76:79], v[68:71], v[88:91]\n\t"
    "v_mfma_f32_16x16x32_bf16 v[92:95], v[80:83], v[68:71], v[92:95]\n\t"
    "ds_read_b128 v[36:39], v11 offset:8192\n\t"
    "ds_read_b128 v[40:43], v11 offset:9216\n\t"
    "v_mov_b32 v44, 0xf149f2ca\n\t"
    "s_nop 4\n\t"
    "v_cndmask_b32_e64 v84, v84, v44, s[78:79]\n\t"
    "v_cndmask_b32_e64 v88, v88, v44, s[86:87]\n\t"
    "v_cndmask_b32_e64 v85, v85, v44, s[80:81]\n\t"
    "v_cndmask_b32_e64 v89, v89, v44, s[88:89]\n\t"
    "v_cndmask_b32_e64 v86, v86, v44, s[82:83]\n\t"
    "v_cndmask_b32_e64 v90, v90, v44, s[90:91]\n\t"
    "v_cndmask_b32_e64 v87, v87, v44, s[84:85]\n\t"
    "v_cndmask_b32_e64 v91, v91, v44, s[92:93]\n\t"
    "v_max3_f32 v20, v84, v85, v86\n\t"
    "v_max3_f32 v21, v87, v88, v89\n\t"
    "v_max3_f32 v22, v90, v91, v92\n\t"
    "v_max3_f32 v23, v93, v94, v95\n\t"
    "v_max3_f32 v20, v20, v21, v22\n\t"
    "v_max_f32 v20, v20, v23\n\t"
    "v_mov_b32 v21, v20\n\t"
    "s_nop 0\n\t"
    "s_nop 0\n\t"
    "v_permlane16_swap_b32 v20, v21\n\t"
    "v_max_f32 v20, v20, v21\n\t"
    "v_mov_b32 v21, v20\n\t"
    "s_nop 0\n\t"
    "s_nop 0\n\t"
    "v_permlane32_swap_b32 v20, v21\n\t"
    "v_max_f32 v20, v20, v21\n\t"
    "v_mul_f32 v22, s98, v20\n\t"
    "v_pk_fma_f32 v[92:93], v[92:93], s[98:99], v[22:23] op_sel_hi:[1,0,0] neg_lo:[0,0,1] neg_hi:[0,0,1]\n\t"
    "v_pk_fma_f32 v[94:95], v[94:95], s[98:99], v[22:23] op_sel_hi:[1,0,0] neg_lo:[0,0,1] neg_hi:[0,0,1]\n\t"
    "v_pk_fma_f32 v[84:85], v[84:85], s[98:99], v[22:23] op_sel_hi:[1,0,0] neg_lo:[0,0,1] neg_hi:[0,0,1]\n\t"
    "v_exp_f32 v92, v92\n\t"
    "v_exp_f32 v93, v93\n\t"
    "v_pk_fma_f32 v[86:87], v[86:87], s[98:99], v[22:23] op_sel_hi:[1,0,0] neg_lo:[0,0,1] neg_hi:[0,0,1]\n\t"
    "v_exp_f32 v94, v94\n\t"
    "v_exp_f32 v95, v95\n\t"
    "v_pk_fma_f32 v[88:89], v[88:89], s[98:99], v[22:23] op_sel_hi:[1,0,0] neg_lo:[0,0,1] neg_hi:[0,0,1]\n\t"
    "v_exp_f32 v84, v84\n\t"
    "v_exp_f32 v85, v85\n\t"
    "v_cvt_pk_bf16_f32 v24, v92, v93\n\t"
    "v_cvt_pk_bf16_f32 v25, v94, v95\n\t"
    "v_pk_fma_f32 v[90:91], v[90:91], s[98:99], v[22:23] op_sel_hi:[1,0,0] neg_lo:[0,0,1] neg_hi:[0,0,1]\n\t"
    "v_exp_f32 v86, v86\n\t"
    "v_exp_f32 v87, v87\n\t"
    "v_mfma_f32_16x16x16_bf16 v[68:71], v[16:17], v[24:25], 0\n\t"
    "v_mfma_f32_16x16x16_bf16 v[72:75], v[18:19], v[24:25], 0\n\t"
    "v_exp_f32 v88, v88\n\t"
    "v_exp_f32 v89, v89\n\t"
    "v_exp_f32 v90, v90\n\t"
    "v_exp_f32 v91, v91\n\t"
    "v_cvt_pk_bf16_f32 v28, v84, v85\n\t"
    "v_cvt_pk_bf16_f32 v29, v86, v87\n\t"
    "v_cvt_pk_bf16_f32 v30, v88, v89\n\t"
    "v_cvt_pk_bf16_f32 v31, v90, v91\n\t"
    "v_pk_add_f32 v[26:27], v[92:93], v[94:95]\n\t"
    "v_pk_add_f32 v[32:33], v[84:85], v[86:87]\n\t"
    "v_pk_add_f32 v[34:35], v[88:89], v[90:91]\n\t"
    "s_waitcnt lgkmcnt(0)\n\t"
    "v_mfma_f32_16x16x32_bf16 v[68:71], v[36:39], v[28:31], v[68:71]\n\t"
    "v_mfma_f32_16x16x32_bf16 v[72:75], v[40:43], v[28:31], v[72:75]\n\t"
    "v_mov_b64 v[36:37], 0\n\t"
    "v_mov_b64 v[38:39], 0\n\t"
    "v_mov_b64 v[40:41], 0\n\t"
    "v_mov_b64 v[42:43], 0\n\t"
    "v_mov_b64 v[44:45], 0\n\t"
    "v_mov_b64 v[46:47], 0\n\t"
    "v_mov_b64 v[48:49], 0\n\t"
    "v_mov_b64 v[50:51], 0\n\t"
    "v_mov_b64 v[52:53], 0\n\t"
    "v_mov_b64 v[54:55], 0\n\t"
    "v_mov_b64 v[56:57], 0\n\t"
    "v_mov_b64 v[58:59], 0\n\t"
    "v_pk_add_f32 v[32:33], v[32:33], v[34:35]\n\t"
    "v_add_f32 v26, v26, v27\n\t"
    "v_add_f32 v32, v32, v33\n\t"
    "v_fmac_f32 v32, 0.5, v26\n\t"
    "v_mov_b32 v33, v32\n\t"
    "s_nop 1\n\t"
    "v_permlane16_swap_b32 v32, v33\n\t"
    "v_add_f32 v32, v32, v33\n\t"
    "v_mov_b32 v33, v32\n\t"
    "s_nop 1\n\t"
    "v_permlane32_swap_b32 v32, v33\n\t"
    "v_add_f32 v32, v32, v33\n\t"
    "v_rcp_f32 v22, v32\n\t"
    "s_nop 0\n\t"
    "v_pk_mul_f32 v[68:69], v[68:69], v[22:23] op_sel_hi:[1,0]\n\t"
    "v_pk_mul_f32 v[70:71], v[70:71], v[22:23] op_sel_hi:[1,0]\n\t"
    "v_pk_mul_f32 v[72:73], v[72:73], v[22:23] op_sel_hi:[1,0]\n\t"
    "v_pk_mul_f32 v[74:75], v[74:75], v[22:23] op_sel_hi:[1,0]\n\t"
    "v_cvt_pk_bf16_f32 v68, v68, v69\n\t"
    "v_cvt_pk_bf16_f32 v69, v70, v71\n\t"
    "v_cvt_pk_bf16_f32 v70, v72, v73\n\t"
    "v_cvt_pk_bf16_f32 v71, v74, v75\n\t"
    "global_store_dwordx4 v0, v[68:71], s[62:63] sc1\n\t"
    "s_cmp_eq_u32 s40, 63\n\t"
    "s_cbranch_scc1 L_end_%=\n\t"
    "s_add_u32 s46, s46, 0x8000\n\t"
    "s_addc_u32 s47, s47, 0\n\t"
    "s_add_u32 s48, s48, 0x8000\n\t"
    "s_addc_u32 s49, s49, 0\n\t"
    "s_add_u32 s60, s60, 0x8000\n\t"
    "s_addc_u32 s61, s61, 0\n\t"
    "s_add_u32 s36, s36, 0x8000\n\t"
    "s_addc_u32 s37, s37, 0\n\t"
    "s_add_u32 s62, s62, 0x2000\n\t"
    "s_addc_u32 s63, s63, 0\n\t"
    "s_add_u32 s64, s64, 0x2000\n\t"
    "s_addc_u32 s65, s65, 0\n\t"
    "s_add_u32 s66, s66, 0x2000\n\t"
    "s_addc_u32 s67, s67, 0\n\t"
    "s_add_u32 s52, s52, s58\n\t"
    "s_addc_u32 s53, s53, s59\n\t"
    "s_add_u32 s54, s54, s58\n\t"
    "s_addc_u32 s55, s55, s59\n\t"
    "s_add_u32 s56, s56, s58\n\t"
    "s_addc_u32 s57, s57, s59\n\t"
    "s_sub_u32 s58, 0, s58\n\t"
    "s_not_b32 s59, s59\n\t"
    "v_add_u32 v10, s99, v10\n\t"
    "v_add_u32 v11, s99, v11\n\t"
    "s_sub_u32 s68, s68, s99\n\t"
    "s_sub_u32 s69, s69, s99\n\t"
    "s_sub_u32 s99, 0, s99\n\t"
    "s_add_u32 s40, s40, 1\n\t"
    "s_sub_u32 s39, s39, 15\n\t"
    "s_max_i32 s39, s39, s33\n\t"
    "s_lshr_b32 s94, s40, 1\n\t"
    "s_add_u32 s94, s94, 1\n\t"
    "s_and_b32 s94, s94, 1\n\t"
    "s_lshl_b32 s43, s94, 16\n\t"
    "s_branch L_top_%=\n\t"
    "L_end_%=:\n\t"
    "s_waitcnt vmcnt(0)\n\t"
        : "+{v10}"(kvl), "+{v11}"(kvw), "+{v13}"(pfkv), "+{v15}"(par)
        : "{v0}"(lane16), "{v1}"(a_mem), "{v2}"(a_mem ^ 128u), "{v3}"(a_imgw), "{v4}"(a_imgr), "{v5}"(a_oir), "{v6}"(a_oiw), "{v7}"(gpos),
          "{v8}"(pofs), "{v9}"(sidx), "{v12}"(wol), "{v14}"(wvl)
        : CHAIN_CLOBBERS);
  }
  vm_drain();
  wg_barrier();
  if (tid == 0) st_flag(f_ox, (unsigned)NSEG);
}

template <bool KV>
__device__ __forceinline__ void proj_role(const Params& P, int g, int idx) {
  const int tid = threadIdx.x, lane = tid & 63, w = __builtin_amdgcn_readfirstlane(tid >> 6);
  const int h0 = KV ? idx : 2 * idx, h1 = KV ? idx : 2 * idx + 1;
  const __bf16* W0 = KV ? P.wk[g] : P.wq[g];
  const __bf16* W1 = KV ? P.wv[g] : P.wq[g];
  const char* xg = (const char*)P.x[g];
  const unsigned xlane = (unsigned)((lane & 15) * (DM * 2) + (lane >> 4) * 16);
  auto xpiece = [&](int s, int buf, int k) {
    const int f = w + 4 * k, tt = f >= 18 ? 1 : 0, t = f - 18 * tt;
    glds16f<false>(xg + (size_t)(32 * s + 16 * tt) * (DM * 2) + t * 64, xlane, buf * 36864 + f * 1024);
  };
  constexpr unsigned RING = 36864;
  const WBase wb0 = w_base(2 * w, lane, RING), wb1 = w_base(2 * w + 1, lane, RING);
  const unsigned cvoff = chunk_lane_off(w, lane);
  ring_stage<18, 7, 9>(
      [&](auto ic, auto sc) { constexpr int i = decltype(ic)::value; stage_chunk(i < 9 ? W0 : W1, i < 9 ? h0 : h1, i % 9, decltype(sc)::value, cvoff, w, RING); },
      [&]() { for (int k = 0; k < 9; ++k) xpiece(0, 0, k); },
      [&](auto ic, auto sc) {
        constexpr int i = decltype(ic)::value, m = i / 9, t = i % 9, slot = decltype(sc)::value;
        static_for<4>([&](auto jc) {
          constexpr int T = decltype(jc)::value >> 1, hf = decltype(jc)::value & 1, f = (2 * m + T) * 18 + 2 * t + hf;
          read_w32_lit<slot, hf, (f < 60), (f < 60 ? 16 + 4 * f : 112 + 4 * (f - 60))>(T ? wb1 : wb0);
        });
      });
  unsigned* flag = P.ctl + (KV ? C_KV + g * 32 + idx : C_Q + g * 16 + idx) * CW;
  char* o0 = (KV ? P.kx : P.qx) + (size_t)(g * NH + h0) * NSEG * FRAGSEG;
  char* o1 = (KV ? P.vx : P.qx) + (size_t)(g * NH + h1) * NSEG * FRAGSEG;
  for (int k = 0; k < 9; ++k) xpiece(1, 1, k);
  int buf = 0;
  for (int s = 0; s < NSEG; ++s) {
    asm volatile("s_waitcnt vmcnt(13)" ::: "memory");
    wg_barrier();
    if (s > 4 && tid == 0) st_flag(flag, (unsigned)s - 1);
    const int s2 = s + 2 < NSEG ? s + 2 : NSEG - 1;
    const int buf2 = buf == 0 ? 2 : buf - 1;
    f32x4 acc[4][2];
    const unsigned xb = opaque(buf * 36864 + lane * 16);
    bf16x8 xf[36];
#pragma unroll
    for (int i = 0; i < 4; ++i) xf[i] = lds_ld<bf16x8>(xb + ((i & 1) * 18 + (i >> 1)) * 1024);
    SB();
    auto step = [&](auto ic) {
      constexpr int i = decltype(ic)::value, t = i >> 1, tt = i & 1;
      if constexpr (i + 4 < 36) xf[i + 4] = lds_ld<bf16x8>(xb + (((i + 4) & 1) * 18 + ((i + 4) >> 1)) * 1024);
      mfma_w<0 * 18 + t, true, t == 0>(acc[0][tt], xf[i]);
      mfma_w<1 * 18 + t, true, t == 0>(acc[1][tt], xf[i]);
      if constexpr (i % 3 == 1 && i / 3 < 9) xpiece(s2, buf2, i / 3);
      mfma_w<2 * 18 + t, !KV, t == 0>(acc[2][tt], xf[i]);
      mfma_w<3 * 18 + t, !KV, t == 0>(acc[3][tt], xf[i]);
      SB();
    };
    static_for<36>(step);
    mfma_fence(acc[0][0], acc[1][0], acc[2][0], acc[3][0]);
    mfma_fence(acc[0][1], acc[1][1], acc[2][1], acc[3][1]);
#pragma unroll
    for (int tt = 0; tt < 2; ++tt)
      st16_sc1(o0 + (size_t)s * FRAGSEG, (unsigned)((tt * 4 + w) * 1024 + lane * 16),
               u32x4{pk2(acc[0][tt][0], acc[0][tt][1]), pk2(acc[0][tt][2], acc[0][tt][3]), pk2(acc[1][tt][0], acc[1][tt][1]), pk2(acc[1][tt][2], acc[1][tt][3])});
    if (KV) {
#pragma unroll
      for (int T = 0; T < 2; ++T)
        st16_sc1(o1 + (size_t)s * FRAGSEG, (unsigned)((2 * w + T) * 1024 + lane * 16),
                 u32x4{pk2(acc[2 + T][0][0], acc[2 + T][0][1]), pk2(acc[2 + T][0][2], acc[2 + T][0][3]), pk2(acc[2 + T][1][0], acc[2 + T][1][1]), pk2(acc[2 + T][1][2], acc[2 + T][1][3])});
    } else {
#pragma unroll
      for (int tt = 0; tt < 2; ++tt)
        st16_sc1(o1 + (size_t)s * FRAGSEG, (unsigned)((tt * 4 + w) * 1024 + lane * 16),
                 u32x4{pk2(acc[2][tt][0], acc[2][tt][1]), pk2(acc[2][tt][2], acc[2][tt][3]), pk2(acc[3][tt][0], acc[3][tt][1]), pk2(acc[3][tt][2], acc[3][tt][3])});
    }
    if (s < 3) {
      vm_drain();
      wg_barrier();
      if (tid == 0) st_flag(flag, (unsigned)s + 1);
    }
    buf = buf == 2 ? 0 : buf + 1;
  }
  vm_drain();
  wg_barrier();
  if (tid == 0) st_flag(flag, (unsigned)NSEG);
}

__device__ __forceinline__ void oproj_role(const Params& P, int ks, int ns, int set) {
  const int tid = threadIdx.x, lane = tid & 63, w = __builtin_amdgcn_readfirstlane(tid >> 6);
  const int g = ks >> 2, hb = 8 * (ks & 3) + 2 * w;
  unsigned* ctl = P.ctl;
  const int sb = set == 0 ? 0 : NSEG - 3 + set, se = set == 0 ? NSEG - 2 : sb + 1;
  char* const pageP = P.pageP[g];
  char* const pageM = P.pageM[g];
  if (set == 0 && (ks & 3) == 0 && ns == 0 && w < 2 && lane == 0) {
    char* base = w == 0 ? pageP + (1536 << 10) : pageM;
    st_flag((unsigned*)base, 0u); st_flag((unsigned*)(base + 8192), 0u); vm_drain();
    long long best0 = 1 << 30, best1 = 1 << 30;
    for (int rep = 1; rep < 4; ++rep) {
      unsigned* a0 = (unsigned*)(base + rep * 512);
      unsigned* a1 = (unsigned*)(base + 8192 + rep * 512);
      long long t0 = clock64();
      st_flag(a0, 0u); vm_drain();
      long long t1 = clock64();
      st_flag(a1, 0u); vm_drain();
      long long t2 = clock64();
      best0 = min(best0, t1 - t0); best1 = min(best1, t2 - t1);
    }
    int m = (int)(best0 - best1);
    m = m > 400 ? 400 : m < -400 ? -400 : m;
    if ((g ^ w) & 1) m = -m;
    __hip_atomic_fetch_add((GAS unsigned*)(ctl + C_PAR * CW), (unsigned)(m + 1024) + (1u << 16), __ATOMIC_RELAXED, __HIP_MEMORY_SCOPE_AGENT);
  }
  if (set == 0) {
    wait_ge(ctl + C_PAR * CW, 4u << 16, ctl);
    const unsigned parw = __builtin_amdgcn_readfirstlane(ld_flag(ctl + C_PAR * CW));
    const unsigned parP = (((parw & 0xFFFFu) > 4096u ? 1u : 0u) ^ g) & 1u, parM = parP ^ 1u;
    const int o = (ks & 3) * 4 + ns;
    const u32x4 z = {0u, 0u, 0u, 0u}, f = {~0u, ~0u, ~0u, ~0u};
    for (int u = tid; u < 2304; u += 256) st16_sc1(pageP, lmap(o * 36864 + u * 16, parP), z);
    for (int sl = 1 + o; sl < NSEG; sl += 16)
      for (int u = tid; u < 577; u += 256) st16_sc1(pageM, lmap(u * 16, parM) + sl * 32768, f);
    vm_drain();
    wg_barrier();
    if (tid == 0) __hip_atomic_fetch_add((GAS unsigned*)(ctl + (C_INIT + g) * CW), 1u, __ATOMIC_RELAXED, __HIP_MEMORY_SCOPE_AGENT);
  }
  {
    const int r3 = lane / 18, c18 = lane % 18;
    const unsigned myb = w * 36864;
    const int q = lane >> 4, q2 = (lane & 15) >> 2, p = lane & 3;
    const unsigned tb = opaque(myb + (4 * q + q2) * 288 + p * 8);
#pragma unroll
    for (int hh = 0; hh < 2; ++hh) {
      const char* src = (const char*)P.wo[g] + ((size_t)(hb + hh) * DH * DM + ns * 144) * 2 + c18 * 16;
      for (int i = 0; i < 43; ++i) {
        const int e = 3 * i + r3;
        if (lane < 54 && e < DH) glds16<false>(src + (size_t)e * (DM * 2), myb + i * 864);
      }
      vm_drain();
      static_for<36>([&](auto ic) {
        constexpr int i = decltype(ic)::value, u = i / 9, n = i % 9;
        auto ld = [&](auto hc) {
          constexpr int f = (decltype(hc)::value * 4 + u) * 9 + n, off = 32 * u * 288 + n * 32;
          constexpr int R = f < 60 ? 16 + 4 * f : 112 + 4 * (f - 60);
          tr_read_lit<(f < 60), R, off>(tb);
          tr_read_lit<(f < 60), R + 2, off + 16 * 288>(tb);
        };
        if (hh == 0) ld(IC<0>{}); else ld(IC<1>{});
      });
      asm volatile("s_waitcnt lgkmcnt(0)" ::: "memory");
    }
  }
  wg_barrier();
  const unsigned* f_ox = ctl + (C_OX + g * 64 + 2 * hb + (lane & 3)) * CW;
  unsigned* f_part = ctl + (C_PART + set * 32 + ks * 4 + ns) * CW;
  const unsigned* f_pr = ctl + (C_PART + set * 32 + (lane & 7) * 4 + ns) * CW;
  const int tt3 = ks >> 2, q3 = ks & 3;

  constexpr unsigned L_GATE = 81920;
  {
    const int tk = lane >> 3, c8 = lane & 7;
    bf16x8 gw[9];
#pragma unroll
    for (int i = 0; i < 9; ++i) gw[i] = *(const bf16x8*)(P.gate + (c8 + 8 * i) * 8);
    for (int b8 = sb >> 3; b8 <= (se - 1) >> 3; ++b8) {
      const int sg = b8 * 8 + tk;
      const __bf16* xr = P.x[0] + (size_t)(32 * sg + 16 * tt3 + 4 * q3 + w) * DM + c8 * 8;
      bf16x8 xv[9];
#pragma unroll
      for (int i = 0; i < 9; ++i) xv[i] = *(const bf16x8*)(xr + 64 * i);
      float a = 0.f;
#pragma unroll
      for (int i = 0; i < 9; ++i) {
        asm volatile("" : "+v"(gw[i]));
#pragma unroll
        for (int e = 0; e < 8; ++e) a += (float)xv[i][e] * (float)gw[i][e];
      }
      a += __shfl_xor(a, 1); a += __shfl_xor(a, 2); a += __shfl_xor(a, 4);
      if (c8 == 0) lds_st<float>(L_GATE + (sg * 4 + w) * 4, 1.0f / (1.0f + __expf(-a)));
    }
  }
  wg_barrier();
  const int fit = tid < 144 ? tid : 143;
  const unsigned foff = (unsigned)((tt3 * 9 + (fit >> 4)) * 1024 + (q3 * 16 + (fit & 15)) * 16);
  auto fin_load = [&](int s) {
    const char* pb = uniform_ptr(P.part + ((size_t)s * 32 + ns) * PARTSEG);
    static_for<8>([&](auto kc) { constexpr int k = decltype(kc)::value; ld16_sc1_lit<224 + 4 * k, 0>(pb + (size_t)k * 4 * PARTSEG, foff); });
  };
  auto fin_store = [&](int s) {
    const int tok0 = 32 * s + 16 * tt3 + 4 * q3;
    const f32x4 gs = lds_ld<f32x4>(L_GATE + s * 16);
    f32x4 a, b;
    asm volatile("v_pk_add_f32 %0, v[224:225], v[228:229]\n\tv_pk_add_f32 %1, v[226:227], v[230:231]\n\t"
                 "v_pk_add_f32 v[232:233], v[232:233], v[236:237]\n\tv_pk_add_f32 v[234:235], v[234:235], v[238:239]\n\t"
                 "v_pk_add_f32 %2, v[240:241], v[244:245]\n\tv_pk_add_f32 %3, v[242:243], v[246:247]\n\t"
                 "v_pk_add_f32 v[248:249], v[248:249], v[252:253]\n\tv_pk_add_f32 v[250:251], v[250:251], v[254:255]\n\t"
                 "v_pk_add_f32 %0, %0, v[232:233]\n\tv_pk_add_f32 %1, %1, v[234:235]\n\t"
                 "v_pk_add_f32 %2, %2, v[248:249]\n\tv_pk_add_f32 %3, %3, v[250:251]"
                 : "=&v"(*(f32x2*)&a), "=&v"(*((f32x2*)&a + 1)), "=&v"(*(f32x2*)&b), "=&v"(*((f32x2*)&b + 1)));
    if (tid < 144) {
#pragma unroll
      for (int r = 0; r < 4; ++r) P.out[(size_t)(tok0 + r) * DM + ns * 144 + fit] = (__bf16)(gs[r] * a[r] + (1.0f - gs[r]) * b[r]);
    }
  };
  auto wait_all = [&](const unsigned* f, unsigned want) {
    for (unsigned sp = 0;; ++sp) {
      asm volatile("" ::: "memory");
      if (__all(ld_flag(f) >= want)) break;
      if ((sp & 63u) == 63u) {
        if (__builtin_amdgcn_readfirstlane(ld_flag(ctl + C_TMO * CW))) break;
        if (sp > SPIN_CAP) { st_flag(ctl + C_TMO * CW, 1u); break; }
      }
      __builtin_amdgcn_s_sleep(1);
    }
  };

  const unsigned lane16 = lane * 16;
  const char* ob0 = uniform_ptr(P.ox + (size_t)(g * NH + hb) * NSEG * FRAGSEG);
  auto aload = [&](auto tc, int s) {
    constexpr int tt = decltype(tc)::value;
    static_for<8>([&](auto fc) {
      constexpr int f = decltype(fc)::value, hh = f >> 2, u = f & 3;
      ld16_sc1_lit<160 + 32 * tt + 4 * f, u * 1024>(ob0 + ((size_t)hh * NSEG + s) * FRAGSEG + tt * 4096, lane16);
    });
  };
  const unsigned redw = opaque(w * 18432 + lane16), redr = opaque(tid * 16);
  bool pref = false;
  if (set != 0) {
    for (unsigned sp = 0; sp < SPIN_CAP; ++sp) {
      if (__all(ld_flag(f_ox) + 2 >= (unsigned)sb + 1)) break;
      __builtin_amdgcn_s_sleep(64);
    }
  }
  for (int s = sb; s < se; ++s) {
    unsigned fnext, fpart;
    asm volatile("global_load_dword %0, %2, off sc1\n\tglobal_load_dword %1, %3, off sc1" : "=&v"(fnext), "=&v"(fpart) : "v"(f_ox), "v"(f_pr) : "memory");
    if (!pref) {
      wait_all(f_ox, (unsigned)s + 1);
      aload(IC<0>{}, s);
      aload(IC<1>{}, s);
    }
    asm volatile("s_waitcnt vmcnt(8)" : "+v"(fnext), "+v"(fpart)::"memory");
    f32x4 acc[2][9];
    static_for<72>([&](auto ic) {
      constexpr int i = decltype(ic)::value, f = i / 9, n = i % 9;
      mfma_ow<160 + 4 * f, f * 9 + n, f == 0>(acc[0][n]);
    });
    asm volatile("s_waitcnt vmcnt(0)" ::: "memory");
    const bool ready = s + 1 < se && __all(fnext >= (unsigned)s + 2);
    const bool fin = s - 2 >= sb, finok = fin && __all(fpart >= (unsigned)s - 1);
    if (ready) aload(IC<0>{}, s + 1);
    if (finok) fin_load(s - 2);
    wg_barrier();
    if (s > sb && tid == 0) st_flag(f_part, (unsigned)s);
    static_for<72>([&](auto ic) {
      constexpr int i = decltype(ic)::value, f = i / 9, n = i % 9;
      mfma_ow<192 + 4 * f, f * 9 + n, f == 0>(acc[1][n]);
      if constexpr (i >= 8 && i % 7 == 1 && (i - 8) / 7 < 9) { lds_st<f32x4>(redw + ((i - 8) / 7) * 1024, acc[0][(i - 8) / 7]); SB(); }
    });
    if (ready) aload(IC<1>{}, s + 1);
    pref = ready;
    asm volatile("s_nop 7" : "+v"(acc[1][0]), "+v"(acc[1][1]), "+v"(acc[1][2]), "+v"(acc[1][3]), "+v"(acc[1][4]), "+v"(acc[1][5]), "+v"(acc[1][6]), "+v"(acc[1][7]), "+v"(acc[1][8]));
#pragma unroll
    for (int n = 0; n < 9; ++n) lds_st<f32x4>(redw + (9 + n) * 1024, acc[1][n]);
    if (fin) {
      if (!finok) {
        wait_all(f_pr, (unsigned)s - 1);
        fin_load(s - 2);
        vm_drain();
      } else if (ready) asm volatile("s_waitcnt vmcnt(8)" ::: "memory");
      else asm volatile("s_waitcnt vmcnt(0)" ::: "memory");
      fin_store(s - 2);
    }
    wg_barrier();
    char* pb = uniform_ptr(P.part + (((size_t)s * 8 + ks) * 4 + ns) * PARTSEG);
    {
      f32x4 x[5][4];
#pragma unroll
      for (int k = 0; k < 5; ++k)
#pragma unroll
        for (int q = 0; q < 4; ++q) x[k][q] = lds_ld<f32x4>(redr + q * 18432 + (k < 4 || tid < 128 ? k * 4096 : 0));
      SB();
#pragma unroll
      for (int k = 0; k < 5; ++k)
        if (k < 4 || tid < 128) st16_sc1(pb, (unsigned)(tid + 256 * k) * 16, __builtin_bit_cast(u32x4, (x[k][0] + x[k][1]) + (x[k][2] + x[k][3])));
    }
  }
  const bool two = se - 2 >= sb;
  if (two) {
    wait_all(f_pr, (unsigned)se - 1);
    fin_load(se - 2);
  }
  vm_drain();
  wg_barrier();
  if (tid == 0) st_flag(f_part, (unsigned)se);
  if (two) fin_store(se - 2);
  wait_all(f_pr, (unsigned)se);
  fin_load(se - 1);
  vm_drain();
  fin_store(se - 1);
}

__global__ void __launch_bounds__(256, 1) __attribute__((amdgpu_num_vgpr(112))) gated_mix_kernel(Params P) {
  asm volatile("" ::: "v255", "a255");
  const int b = blockIdx.x, xs = b & 7, rank = b >> 3, g = xs >> 2;
  if (rank < 16) chain_role(P, g, (xs & 3) * 16 + rank);
  else {
    if (rank < 24) proj_role<true>(P, g, (xs & 3) * 8 + (rank - 16));
    else if (rank < 28) proj_role<false>(P, g, (xs & 3) * 4 + (rank - 24));
    if (rank >= 28 || rank < 24) oproj_role(P, xs, rank & 3, rank >= 28 ? 0 : 1 + ((rank - 16) >> 2));
  }
}

}

extern "C" void kernel_launch(void* const* d_in, const int* in_sizes, int n_in, void* d_out, int out_size, void* d_ws,
                              size_t ws_size, hipStream_t stream) {
  (void)in_sizes; (void)n_in; (void)out_size; (void)ws_size;
  Params P;
  for (int g = 0; g < 2; ++g) {
    const int o = g * 7;
    P.x[g] = (const __bf16*)d_in[0];
    P.mem0[g] = (const __bf16*)d_in[o + 1];
    P.wq[g] = (const __bf16*)d_in[o + 2];
    P.wk[g] = (const __bf16*)d_in[o + 3];
    P.wv[g] = (const __bf16*)d_in[o + 4];
    P.wo[g] = (const __bf16*)d_in[o + 5];
  }
  P.gate = (const __bf16*)d_in[14];
  P.out = (__bf16*)d_out;
  char* ws = (char*)d_ws;
  P.ctl = (unsigned*)ws;
  const uintptr_t pg = ((uintptr_t)ws + 65536 + (2u << 20) - 1) & ~(uintptr_t)((2u << 20) - 1);
  for (int g = 0; g < 2; ++g) {
    P.pageP[g] = (char*)pg + (size_t)(2 * g) * (2u << 20);
    P.pageM[g] = (char*)pg + (size_t)(2 * g + 1) * (2u << 20);
  }
  P.kx = (char*)pg + (size_t)4 * (2u << 20);
  P.vx = P.kx + FRAGALL;
  P.qx = P.vx + FRAGALL;
  P.ox = P.qx + FRAGALL;
  P.part = P.ox + FRAGALL;
  (void)hipFuncSetAttribute((const void*)gated_mix_kernel, hipFuncAttributeMaxDynamicSharedMemorySize, LDS_BYTES);
  (void)hipMemsetAsync(d_ws, 0, CTL_BYTES, stream);
  gated_mix_kernel<<<256, 256, LDS_BYTES, stream>>>(P);
}
```
